```python
import torch

def _softmax(x, axis=-1):
    return torch.softmax(x, dim=axis)

def _gelu(x):
    return torch.nn.functional.gelu(x, approximate='tanh')

def _silu(x):
    return torch.nn.functional.silu(x)

def _sigmoid(x):
    return torch.sigmoid(x)

def _layernorm(x, axis=-1, eps=1e-05):
    m = x.mean(dim=axis, keepdim=True)
    v = x.var(dim=axis, keepdim=True, unbiased=False)
    return (x - m) / torch.sqrt(v + eps)

def _scatter_add(tgt, idx, src, axis=0):
    return tgt.clone().index_add_(axis, idx, src)

def _segment_sum(src, seg, n):
    out = torch.zeros((n,) + src.shape[1:], dtype=src.dtype)
    return out.index_add_(0, seg, src)

def _pad(x, pw):
    flat = tuple((v for pair in reversed(pw) for v in pair))
    return torch.nn.functional.pad(x, flat)
kernel_name = 'adaLN_cond__memory_aug_recurrent_mem_tr__glu_gated_geglu__prompt'

def setup_inputs():

    _w = lambda a: (lambda t: t.to(torch.bfloat16) if t.is_floating_point() else t)(torch.as_tensor(a))

    def _A_setup():
        B, S, Dm = (1, 2048, 640)
        H, Dh = (32, 192)
        M, seg = (8, 32)
        seg = min(seg, S)
        S = S // seg * seg
        return {'x': _w((lambda *s: torch.randn(*s, dtype=torch.bfloat16) * 0.05)(B, S, Dm)), 'mem0': _w((lambda *s: torch.randn(*s, dtype=torch.bfloat16) * 0.8)(B, M, Dm)), 'Wq': _w((lambda *s: torch.randn(*s, dtype=torch.bfloat16) * 0.8)(Dm, H, Dh) * Dm ** (-0.5)), 'Wk': _w((lambda *s: torch.randn(*s, dtype=torch.bfloat16) * 0.8)(Dm, H, Dh) * Dm ** (-0.5)), 'Wv': _w((lambda *s: torch.randn(*s, dtype=torch.bfloat16) * 0.8)(Dm, H, Dh) * Dm ** (-0.5)), 'Wo': _w((lambda *s: torch.randn(*s, dtype=torch.bfloat16) * 0.8)(H, Dh, Dm) * (H * Dh) ** (-0.5)), 'seg_len': seg}
    _A_in = _A_setup()

    def _B_setup():
        B, S, Dm, Df = (1, 2048, 640, 1536)
        return {'x': _w((lambda *s: torch.randn(*s, dtype=torch.bfloat16) * 0.05)(B, S, Dm)), 'w_gate': _w((lambda *s: torch.randn(*s, dtype=torch.bfloat16) * 0.4)(Dm, Df) * Dm ** (-0.5)), 'w_up': _w((lambda *s: torch.randn(*s, dtype=torch.bfloat16) * 0.4)(Dm, Df) * Dm ** (-0.5)), 'w_down': _w((lambda *s: torch.randn(*s, dtype=torch.bfloat16) * 0.4)(Df, Dm) * Df ** (-0.5))}
    _B_in = _B_setup()
    return {'A_x': _A_in['x'], 'A_mem0': _A_in['mem0'], 'A_Wq': _A_in['Wq'], 'A_Wk': _A_in['Wk'], 'A_Wv': _A_in['Wv'], 'A_Wo': _A_in['Wo'], 'A_seg_len': _A_in['seg_len'], 'B_x': _B_in['x'], 'B_w_gate': _B_in['w_gate'], 'B_w_up': _B_in['w_up'], 'B_w_down': _B_in['w_down'], 'c': _w((lambda *s: torch.randn(*s, dtype=torch.bfloat16) * 0.2)(1, 640) * 8.0), 'w_mod': _w((lambda *s: torch.randn(*s, dtype=torch.bfloat16) * 0.2)(640, 6 * 640) * 640 ** (-0.5) * 8.0)}

def reference(A_x, A_mem0, A_Wq, A_Wk, A_Wv, A_Wo, A_seg_len, B_x, B_w_gate, B_w_up, B_w_down, c, w_mod):

    def _A_ref(x, mem0, Wq, Wk, Wv, Wo, seg_len):
        B, S, Dm = x.shape
        H, Dh = (Wq.shape[1], Wq.shape[2])
        M = mem0.shape[1]
        scale = Dh ** (-0.5)
        n_seg = S // seg_len
        L = M + seg_len
        idx = torch.arange(L)
        mask = (idx[:, None] < M) | (idx[:, None] >= idx[None, :])
        mem = mem0
        outs = []
        for s in range(n_seg):
            xs = x[:, s * seg_len:(s + 1) * seg_len, :].clone()
            h = torch.concatenate([mem, xs], axis=1)
            q = torch.einsum('btd,dhe->bhte', h, Wq)
            k = torch.einsum('btd,dhe->bhte', h, Wk)
            v = torch.einsum('btd,dhe->bhte', h, Wv)
            scores = torch.einsum('bhqe,bhke->bhqk', q, k) * scale
            scores = torch.where(mask[None, None], scores, -1000000000.0)
            p = _softmax(scores, axis=-1)
            o = torch.einsum('bhqk,bhke->bhqe', p, v)
            o = torch.einsum('bhte,hed->btd', o, Wo)
            mem = o[:, :M, :].clone()
            outs.append(o[:, M:, :])
        return torch.concatenate(outs, axis=1)

    def _B_ref(x, w_gate, w_up, w_down):
        g = torch.einsum('bsd,df->bsf', x, w_gate)
        u = torch.einsum('bsd,df->bsf', x, w_up)
        h = _gelu(g) * u
        return torch.einsum('bsf,fd->bsd', h, w_down)
    x = A_x
    mod = (_silu(c) @ w_mod).reshape(c.shape[0], 6, 1, -1)
    sa, ca, ga, sb, cb, gb = (mod[:, 0].clone(), mod[:, 1].clone(), mod[:, 2].clone(), mod[:, 3].clone(), mod[:, 4].clone(), mod[:, 5].clone())
    h = _layernorm(x, axis=-1) * (1 + ca) + sa
    x = x + ga * _A_ref(h, A_mem0, A_Wq, A_Wk, A_Wv, A_Wo, A_seg_len)
    h = _layernorm(x, axis=-1) * (1 + cb) + sb
    x = x + gb * _B_ref(h, B_w_gate, B_w_up, B_w_down)
    return x
```

```cpp
#include <hip/hip_runtime.h>
#include <hip/hip_bf16.h>
#include <stdint.h>
#include <utility>

typedef __bf16 bf16x2 __attribute__((ext_vector_type(2)));
typedef __bf16 bf16x8 __attribute__((ext_vector_type(8)));
typedef __bf16 bf16x16 __attribute__((ext_vector_type(16)));
typedef float f32x2 __attribute__((ext_vector_type(2)));
typedef float f32x4 __attribute__((ext_vector_type(4)));
typedef unsigned u32x2 __attribute__((ext_vector_type(2)));
typedef unsigned u32x4 __attribute__((ext_vector_type(4)));
typedef unsigned u32x8 __attribute__((ext_vector_type(8)));
typedef unsigned short u16;
typedef __amdgpu_buffer_rsrc_t rsrc_t;
#define LAS __attribute__((address_space(3)))
#define DEV __device__ __forceinline__

constexpr int S_ = 2048, D_ = 640, H_ = 32, DH = 192, HD = 6144, NSEG = 64, DF = 1536;
constexpr int AUX_SC1 = 16;
constexpr unsigned SENT = 0xFFFFFFFFu;
constexpr unsigned SPIN_CAP = 1u << 20;

constexpr size_t OFF_CTRL = 0;
constexpr size_t OFF_MOD = 4096;
constexpr size_t OFF_MEMB = 32768;
constexpr size_t SZ_MEMB = (size_t)NSEG * 10240;
constexpr size_t OFF_PB = OFF_MEMB + SZ_MEMB;
constexpr size_t SZ_PB = (size_t)NSEG * 20480;
constexpr size_t OFF_VOM = OFF_PB + SZ_PB;
constexpr size_t SZ_VOM = (size_t)NSEG * 40 * 32 * 256;
constexpr size_t FILL_BEG = OFF_MEMB + 10240, FILL_END = OFF_VOM + SZ_VOM;
constexpr size_t OFF_HF = FILL_END;
constexpr size_t SZ_HF = (size_t)S_ * D_ * 2;
constexpr size_t OFF_KX = OFF_HF + SZ_HF;
constexpr size_t SZ_KX = (size_t)S_ * HD * 2;
constexpr size_t OFF_VOX = OFF_KX + SZ_KX;
constexpr size_t SZ_VOX = (size_t)NSEG * 40 * 32 * 1024;
constexpr size_t OFF_KM = OFF_VOX + SZ_VOX;
constexpr size_t SZ_KM = (size_t)NSEG * 32 * 8 * 192 * 2;
constexpr size_t OFF_PX = OFF_KM + SZ_KM;
constexpr size_t SZ_PX = (size_t)NSEG * 2 * 40 * 1024;
constexpr size_t OFF_X1 = OFF_PX + SZ_PX;
constexpr size_t SZ_X1 = (size_t)S_ * D_ * 4;
constexpr size_t OFF_H2 = OFF_X1 + SZ_X1;
constexpr size_t OFF_ACT = OFF_H2 + SZ_HF;
constexpr size_t SZ_ACT = (size_t)S_ * DF * 2;
constexpr size_t WS_END = OFF_ACT + SZ_ACT;
static_assert(WS_END <= (256u << 20), "ws");

struct Params {
  const u16 *x, *mem0, *wq, *wk, *wv, *wo, *wg, *wu, *wd, *c, *wmod;
  u16* out;
  unsigned char* ws;
};

DEV rsrc_t mkrsrc(const void* p, unsigned bytes) { return __builtin_amdgcn_make_buffer_rsrc((void*)p, 0, (int)bytes, 0x00020000); }
DEV float bf2f(unsigned b) { return __builtin_bit_cast(float, b << 16); }
DEV float bflo(unsigned d) { return __builtin_bit_cast(float, d << 16); }
DEV float bfhi(unsigned d) { return __builtin_bit_cast(float, d & 0xFFFF0000u); }
DEV unsigned pack2(float a, float b) { f32x2 v = {a, b}; return __builtin_bit_cast(unsigned, __builtin_convertvector(v, bf16x2)); }
DEV unsigned nosent(unsigned d) { return d > 0xFFFFFFFEu ? 0xFFFFFFFEu : d; }
DEV float rbf(float a) { return bflo(pack2(a, 0.f) & 0xFFFFu); }
DEV f32x4 mfma(u32x4 a, u32x4 b, f32x4 c) {
  return __builtin_amdgcn_mfma_f32_16x16x32_bf16(__builtin_bit_cast(bf16x8, a), __builtin_bit_cast(bf16x8, b), c, 0, 0, 0);
}
DEV f32x4 smf(u32x4 a, u32x8 b, f32x4 c, int idx) {
  return __builtin_amdgcn_smfmac_f32_16x16x64_bf16(__builtin_bit_cast(bf16x8, a), __builtin_bit_cast(bf16x16, b), c, idx, 0, 0);
}
DEV u32x4 lo4(u32x8 v) { return __builtin_shufflevector(v, v, 0, 1, 2, 3); }
DEV u32x4 hi4(u32x8 v) { return __builtin_shufflevector(v, v, 4, 5, 6, 7); }
DEV u32x8 cat8(u32x4 a, u32x4 b) { return __builtin_shufflevector(a, b, 0, 1, 2, 3, 4, 5, 6, 7); }
DEV void wg_barrier() { asm volatile("s_waitcnt lgkmcnt(0)\n\ts_barrier" ::: "memory"); }
DEV bool valid4(u32x4 v) { return v.x != SENT && v.y != SENT && v.z != SENT && v.w != SENT; }
DEV bool valid2(u32x2 v) { return v.x != SENT && v.y != SENT; }
DEV float swap_add32(float a, float b) {
  auto r = __builtin_amdgcn_permlane32_swap(__builtin_bit_cast(unsigned, a), __builtin_bit_cast(unsigned, b), false, false);
  return __builtin_bit_cast(float, (unsigned)r[0]) + __builtin_bit_cast(float, (unsigned)r[1]);
}
DEV float max_g(float x) {
  unsigned u = __builtin_bit_cast(unsigned, x);
  auto r = __builtin_amdgcn_permlane32_swap(u, u, false, false);
  x = fmaxf(__builtin_bit_cast(float, (unsigned)r[0]), __builtin_bit_cast(float, (unsigned)r[1]));
  u = __builtin_bit_cast(unsigned, x);
  auto q = __builtin_amdgcn_permlane16_swap(u, u, false, false);
  return fmaxf(__builtin_bit_cast(float, (unsigned)q[0]), __builtin_bit_cast(float, (unsigned)q[1]));
}
DEV float sum_g(float x) {
  unsigned u = __builtin_bit_cast(unsigned, x);
  auto r = __builtin_amdgcn_permlane32_swap(u, u, false, false);
  x = __builtin_bit_cast(float, (unsigned)r[0]) + __builtin_bit_cast(float, (unsigned)r[1]);
  u = __builtin_bit_cast(unsigned, x);
  auto q = __builtin_amdgcn_permlane16_swap(u, u, false, false);
  return __builtin_bit_cast(float, (unsigned)q[0]) + __builtin_bit_cast(float, (unsigned)q[1]);
}
DEV u32x4 lds_r128(LAS unsigned char* L, int off) { return *(LAS u32x4*)(L + off); }
DEV void lds_w128(LAS unsigned char* L, int off, u32x4 v) { *(LAS u32x4*)(L + off) = v; }

template <bool PERM> DEV int lane_row(int gb) { return PERM ? (gb >> 1) + 80 * (gb & 1) : 8 * gb; }
template <bool PERM> constexpr int const_row(int u, int jj) { return PERM ? 160 * (u / 5) + 2 * (u % 5) + 10 * jj : 32 * u + jj; }

template <bool PERM, int LDW>
DEV void load3_issue(rsrc_t rs, int voff, int u, u32x2 (&d)[8]) {
#pragma unroll
  for (int jj = 0; jj < 8; ++jj) d[jj] = __builtin_amdgcn_raw_buffer_load_b64(rs, voff, const_row<PERM>(u, jj) * LDW * 2, 0);
}
DEV void load3_finish(const u32x2 (&d)[8], unsigned sh, u32x4 (&f)[3]) {
  unsigned lo[8], hi[8];
#pragma unroll
  for (int jj = 0; jj < 8; ++jj) { lo[jj] = __builtin_amdgcn_alignbit(d[jj].y, d[jj].x, sh); hi[jj] = d[jj].y >> sh; }
#pragma unroll
  for (int q = 0; q < 4; ++q) {
    f[0][q] = __builtin_amdgcn_perm(lo[2 * q + 1], lo[2 * q], 0x05040100u);
    f[1][q] = __builtin_amdgcn_perm(lo[2 * q + 1], lo[2 * q], 0x07060302u);
    f[2][q] = __builtin_amdgcn_perm(hi[2 * q + 1], hi[2 * q], 0x05040100u);
  }
}
template <bool PERM, int LDW>
DEV void load3(rsrc_t rs, int voff, unsigned sh, int u, u32x4 (&f)[3]) {
  u32x2 d[8];
  load3_issue<PERM, LDW>(rs, voff, u, d);
  load3_finish(d, sh, f);
}
struct Raw10 { u32x4 a[8]; unsigned b[8]; };
template <int LDW>
DEV void load10_issue(rsrc_t rs, int voff, int u, Raw10& r) {
#pragma unroll
  for (int jj = 0; jj < 8; ++jj) {
    r.a[jj] = __builtin_amdgcn_raw_buffer_load_b128(rs, voff, (32 * u + jj) * LDW * 2, 0);
    r.b[jj] = __builtin_amdgcn_raw_buffer_load_b32(rs, voff + 16, (32 * u + jj) * LDW * 2, 0);
  }
}
DEV void load10_finish(const Raw10& r, u32x4 (&f)[10]) {
#pragma unroll
  for (int t = 0; t < 10; ++t)
#pragma unroll
    for (int q = 0; q < 4; ++q) {
      const unsigned hi = t < 8 ? r.a[2 * q + 1][(t >> 1) & 3] : r.b[2 * q + 1];
      const unsigned lo = t < 8 ? r.a[2 * q][(t >> 1) & 3] : r.b[2 * q];
      f[t][q] = __builtin_amdgcn_perm(hi, lo, (t & 1) ? 0x07060302u : 0x05040100u);
    }
}
template <int LDW>
DEV void load10(rsrc_t rs, int voff, int u, u32x4 (&f)[10]) {
  Raw10 r;
  load10_issue<LDW>(rs, voff, u, r);
  load10_finish(r, f);
}

__global__ void __launch_bounds__(256) k_pro(Params p) {
  __shared__ float red[32][64 + 1];
  const int tid = threadIdx.x, b = blockIdx.x;
  if (b < 60) {
    const int ct = tid & 7, ks = tid >> 3;
    const int col = b * 64 + ct * 8;
    float acc[8];
#pragma unroll
    for (int i = 0; i < 8; ++i) acc[i] = 0.f;
    u32x4 w[20];
#pragma unroll
    for (int k = 0; k < 20; ++k) w[k] = *(const u32x4*)(p.wmod + (size_t)(ks * 20 + k) * 3840 + col);
#pragma unroll
    for (int k = 0; k < 20; ++k) {
      float cv = bf2f(p.c[ks * 20 + k]);
      float sv = rbf(cv / (1.f + __expf(-cv)));
#pragma unroll
      for (int q = 0; q < 4; ++q) { acc[2 * q] += sv * bflo(w[k][q]); acc[2 * q + 1] += sv * bfhi(w[k][q]); }
    }
#pragma unroll
    for (int i = 0; i < 8; ++i) red[ks][ct * 8 + i] = acc[i];
    __syncthreads();
    if (tid < 64) {
      float s = 0.f;
#pragma unroll
      for (int k = 0; k < 32; ++k) s += red[k][tid];
      ((float*)(p.ws + OFF_MOD))[b * 64 + tid] = rbf(s);
    }
  } else if (b == 60) {
    u16* dst = (u16*)(p.ws + OFF_MEMB);
    for (int idx = tid; idx < 5120; idx += 256) {
      int j = idx >> 7, r = idx & 127, half = r >> 6, i = (r >> 3) & 7, d = (r >> 1) & 3, e = r & 1;
      int n = 4 * d + 2 * half + e;
      int col = 160 * (j / 10) + 10 * n + j % 10;
      u16 v = p.mem0[i * D_ + col];
      dst[idx] = v == 0xFFFFu ? (u16)0xFFFEu : v;
    }
    if (tid < 64) ((unsigned*)(p.ws + OFF_CTRL))[tid] = 0u;
  } else {
    const size_t n16 = (FILL_END - FILL_BEG) / 16;
    u32x4* dst = (u32x4*)(p.ws + FILL_BEG);
    const u32x4 sv = {SENT, SENT, SENT, SENT};
    for (size_t i = (size_t)(b - 61) * 256 + tid; i < n16; i += (size_t)(gridDim.x - 61) * 256) dst[i] = sv;
  }
}

template <bool F32IN>
__global__ void __launch_bounds__(256) k_ln(Params p) {
  const int tid = threadIdx.x, tile = blockIdx.x;
  const int row16 = tid >> 4, l16 = tid & 15;
  const int row = tile * 16 + row16;
  const float* mod = (const float*)(p.ws + OFF_MOD);
  const float* sh = mod + (F32IN ? 3 * D_ : 0);
  const float* sc = mod + (F32IN ? 4 * D_ : 1 * D_);
  float v[5][8];
#pragma unroll
  for (int q = 0; q < 5; ++q) {
    const int unit = l16 + 16 * q;
    if (F32IN) {
      const f32x4* src = (const f32x4*)(p.ws + OFF_X1 + ((size_t)row * D_ + unit * 8) * 4);
      f32x4 a = src[0], b = src[1];
      v[q][0] = a.x; v[q][1] = a.y; v[q][2] = a.z; v[q][3] = a.w; v[q][4] = b.x; v[q][5] = b.y; v[q][6] = b.z; v[q][7] = b.w;
    } else {
      u32x4 a = *(const u32x4*)(p.x + (size_t)row * D_ + unit * 8);
#pragma unroll
      for (int i = 0; i < 4; ++i) { v[q][2 * i] = bflo(a[i]); v[q][2 * i + 1] = bfhi(a[i]); }
    }
  }
  float s = 0.f;
#pragma unroll
  for (int q = 0; q < 5; ++q)
#pragma unroll
    for (int i = 0; i < 8; ++i) s += v[q][i];
#pragma unroll
  for (int o = 1; o < 16; o <<= 1) s += __shfl_xor(s, o);
  const float mean = s * (1.f / D_);
  float ss = 0.f;
#pragma unroll
  for (int q = 0; q < 5; ++q)
#pragma unroll
    for (int i = 0; i < 8; ++i) { float d = v[q][i] - mean; ss += d * d; }
#pragma unroll
  for (int o = 1; o < 16; o <<= 1) ss += __shfl_xor(ss, o);
  const float rstd = 1.f / sqrtf(ss * (1.f / D_) + 1e-5f);
  unsigned char* dst = p.ws + (F32IN ? OFF_H2 : OFF_HF) + (size_t)tile * 20480;
#pragma unroll
  for (int q = 0; q < 5; ++q) {
    const int unit = l16 + 16 * q;
    float o[8];
#pragma unroll
    for (int i = 0; i < 8; ++i) o[i] = (v[q][i] - mean) * rstd * (1.f + sc[unit * 8 + i]) + sh[unit * 8 + i];
    u32x4 r = {pack2(o[0], o[1]), pack2(o[2], o[3]), pack2(o[4], o[5]), pack2(o[6], o[7])};
    *(u32x4*)(dst + (unit >> 2) * 1024 + ((unit & 3) * 16 + row16) * 16) = r;
  }
}

template <int I> struct IC { static constexpr int v = I; };
template <class F, int... Is> DEV void sfor_impl(F&& f, std::integer_sequence<int, Is...>) { (f(IC<Is>{}), ...); }
template <int N, class F> DEV void sfor(F&& f) { sfor_impl(f, std::make_integer_sequence<int, N>{}); }

constexpr int NAG = 32, NVG = 13, NLDS = 15;
constexpr int WL_BYTES = 4 * NLDS * 2048;
enum { LOC_A = 0, LOC_V = 1, LOC_L = 2 };
constexpr int f_loc(int f) { return (f & 3) == 3 ? LOC_L : ((f - ((f + 1) >> 2)) < NAG ? LOC_A : LOC_V); }
constexpr int f_idx(int f) { return (f & 3) == 3 ? (f >> 2) : ((f - ((f + 1) >> 2)) < NAG ? (f - ((f + 1) >> 2)) : (f - ((f + 1) >> 2)) - NAG); }
struct WSet { u32x8 a[NAG]; u32x8 v[NVG]; };
struct WSetD { u32x4 a[NAG][2]; u32x4 v[NVG][2]; };

template <int F> DEV void wput(WSet& W, LAS unsigned char* L, int wl, u32x4 x, u32x4 y) {
  if constexpr (f_loc(F) == LOC_L) { lds_w128(L, wl + f_idx(F) * 2048, x); lds_w128(L, wl + f_idx(F) * 2048 + 1024, y); }
  else if constexpr (f_loc(F) == LOC_A) { W.a[f_idx(F)] = cat8(x, y); asm volatile("" : "+a"(W.a[f_idx(F)])); }
  else { W.v[f_idx(F)] = cat8(x, y); asm volatile("" : "+v"(W.v[f_idx(F)])); }
}
template <int F> DEV void wput(WSetD& W, LAS unsigned char* L, int wl, u32x4 x, u32x4 y) {
  if constexpr (f_loc(F) == LOC_L) { lds_w128(L, wl + f_idx(F) * 2048, x); lds_w128(L, wl + f_idx(F) * 2048 + 1024, y); }
  else if constexpr (f_loc(F) == LOC_A) { W.a[f_idx(F)][0] = x; W.a[f_idx(F)][1] = y; asm volatile("" : "+a"(W.a[f_idx(F)][0]), "+a"(W.a[f_idx(F)][1])); }
  else { W.v[f_idx(F)][0] = x; W.v[f_idx(F)][1] = y; asm volatile("" : "+v"(W.v[f_idx(F)][0]), "+v"(W.v[f_idx(F)][1])); }
}
struct RingD { u32x4 h[2]; };
DEV RingD wldsD(LAS unsigned char* L, int wl, int li) { RingD r; r.h[0] = lds_r128(L, wl + li * 2048); r.h[1] = lds_r128(L, wl + li * 2048 + 1024); return r; }
DEV u32x8 wlds(LAS unsigned char* L, int wl, int li) { return cat8(lds_r128(L, wl + li * 2048), lds_r128(L, wl + li * 2048 + 1024)); }

#define SMF_A(c, a, b, i) asm volatile("v_smfmac_f32_16x16x64_bf16 %0, %1, %2, %3" : "+v"(c) : "v"(a), "a"(b), "v"(i))
#define SMF_V(c, a, b, i) asm volatile("v_smfmac_f32_16x16x64_bf16 %0, %1, %2, %3" : "+v"(c) : "v"(a), "v"(b), "v"(i))
template <int F> DEV void smfw(f32x4& c, const u32x4& a, const WSet& W, const u32x8& lb, int idx) {
  if constexpr (f_loc(F) == LOC_A) SMF_A(c, a, W.a[f_idx(F)], idx);
  else if constexpr (f_loc(F) == LOC_V) SMF_V(c, a, W.v[f_idx(F)], idx);
  else SMF_V(c, a, lb, idx);
}
#define MF_AV(c, a, b) asm volatile("v_mfma_f32_16x16x32_bf16 %0, %1, %2, %0" : "+v"(c) : "a"(a), "v"(b))
#define MF_VA(c, a, b) asm volatile("v_mfma_f32_16x16x32_bf16 %0, %1, %2, %0" : "+v"(c) : "v"(a), "a"(b))
#define MF_VV(c, a, b) asm volatile("v_mfma_f32_16x16x32_bf16 %0, %1, %2, %0" : "+v"(c) : "v"(a), "v"(b))
template <int F, bool WA, bool HI> DEV void mfw(f32x4& c, const u32x4& x, const WSetD& W, const RingD& lb) {
  if constexpr (f_loc(F) == LOC_A) {
    if constexpr (WA) MF_AV(c, W.a[f_idx(F)][HI], x); else MF_VA(c, x, W.a[f_idx(F)][HI]);
  } else if constexpr (f_loc(F) == LOC_V) {
    if constexpr (WA) MF_VV(c, W.v[f_idx(F)][HI], x); else MF_VV(c, x, W.v[f_idx(F)][HI]);
  } else {
    if constexpr (WA) MF_VV(c, lb.h[HI], x); else MF_VV(c, x, lb.h[HI]);
  }
}
#define NOP_IN3(a, b, c) asm volatile("s_nop 1" : "+v"(a), "+v"(b), "+v"(c))
#define NOP_OUT3(a, b, c) asm volatile("s_nop 7" : "+v"(a), "+v"(b), "+v"(c))
#define NOP_IN2(a, b) asm volatile("s_nop 1" : "+v"(a), "+v"(b))
#define NOP_OUT2(a, b) asm volatile("s_nop 7" : "+v"(a), "+v"(b))

template <bool PERM, int FB, int FS, class WS>
DEV void load_proj(const u16* Wm, int h, int w, int lane, WS& W, LAS unsigned char* L, int wl) {
  const rsrc_t rs = mkrsrc(Wm, D_ * HD * 2);
  const int nl = lane & 15, gb = lane >> 4;
  const int col = h * DH + 48 * w + 3 * nl;
  const int voff = (lane_row<PERM>(gb) * HD + (col & ~1)) * 2;
  const unsigned sh = (col & 1) * 16;
  u32x2 raw[3][2][8];
  load3_issue<PERM, HD>(rs, voff, 0, raw[0][0]); load3_issue<PERM, HD>(rs, voff, 1, raw[0][1]);
  load3_issue<PERM, HD>(rs, voff, 2, raw[1][0]); load3_issue<PERM, HD>(rs, voff, 3, raw[1][1]);
  sfor<10>([&](auto T_) {
    constexpr int T = decltype(T_)::v;
    if constexpr (T + 2 < 10) {
      load3_issue<PERM, HD>(rs, voff, 2 * T + 4, raw[(T + 2) % 3][0]);
      load3_issue<PERM, HD>(rs, voff, 2 * T + 5, raw[(T + 2) % 3][1]);
    }
    asm volatile("" ::: "memory");
    u32x4 a[3], b[3];
    load3_finish(raw[T % 3][0], sh, a);
    load3_finish(raw[T % 3][1], sh, b);
    sfor<3>([&](auto t_) { constexpr int t = decltype(t_)::v; wput<FB + T * FS + t>(W, L, wl, a[t], b[t]); });
  });
}
template <int FB, class WS>
DEV void load_wo(const u16* Wm, int h, int w, int lane, WS& W, LAS unsigned char* L, int wl) {
  const rsrc_t rs = mkrsrc(Wm, D_ * HD * 2);
  const int nl = lane & 15, gb = lane >> 4;
  const int voff = ((h * DH + 8 * gb) * D_ + 160 * w + 10 * nl) * 2;
  sfor<3>([&](auto T_) {
    constexpr int T = decltype(T_)::v;
    Raw10 r0, r1;
    load10_issue<D_>(rs, voff, 2 * T, r0);
    load10_issue<D_>(rs, voff, 2 * T + 1, r1);
    asm volatile("" ::: "memory");
    u32x4 a[10], b[10];
    load10_finish(r0, a);
    load10_finish(r1, b);
    sfor<10>([&](auto t_) { constexpr int t = decltype(t_)::v; wput<FB + (t / 2) * 6 + T * 2 + (t & 1)>(W, L, wl, a[t], b[t]); });
  });
}
constexpr int NRING = 3;

constexpr int L_W = 0;
constexpr int L_MEM = WL_BYTES;
constexpr int L_QK = L_MEM + 10240;
constexpr int L_KX = L_QK + 6400;
constexpr int L_END = L_KX + 12288;
constexpr int QROW = 400;

DEV bool gather_mem(rsrc_t rws, int s, int w, int lane, LAS unsigned char* L, unsigned* ctrl) {
  const int base = (int)OFF_MEMB + s * 10240 + lane * 16;
  const int c2 = w < 2 ? w + 8 : w;
  unsigned spins = 0;
  for (;;) {
    asm volatile("" ::: "memory");
    u32x4 v0 = __builtin_amdgcn_raw_buffer_load_b128(rws, base + w * 1024, 0, AUX_SC1);
    u32x4 v1 = __builtin_amdgcn_raw_buffer_load_b128(rws, base + (w + 4) * 1024, 0, AUX_SC1);
    u32x4 v2 = __builtin_amdgcn_raw_buffer_load_b128(rws, base + c2 * 1024, 0, AUX_SC1);
    bool ok = valid4(v0) && valid4(v1) && valid4(v2);
    if (__all(ok)) {
      lds_w128(L, L_MEM + w * 1024 + lane * 16, v0);
      lds_w128(L, L_MEM + (w + 4) * 1024 + lane * 16, v1);
      lds_w128(L, L_MEM + c2 * 1024 + lane * 16, v2);
      return true;
    }
    if (++spins > SPIN_CAP) { if (lane == 0) ctrl[0] = 1u; return false; }
  }
}

DEV void qk_role(const Params& p, int h, LAS unsigned char* L) {
  const int tid = threadIdx.x, lane = tid & 63, w = __builtin_amdgcn_readfirstlane(tid >> 6);
  const int nl = lane & 15, g = lane >> 4;
  const int wl = L_W + w * (NLDS * 2048) + lane * 16;
  unsigned* ctrl = (unsigned*)(p.ws + OFF_CTRL);
  WSet W;
  load_proj<true, 0, 6>(p.wq, h, w, lane, W, L, wl);
  load_proj<true, 3, 6>(p.wk, h, w, lane, W, L, wl);
  const rsrc_t rws = mkrsrc(p.ws, (unsigned)WS_END);
  const int idx = (nl < 8) ? 0x4444 : 0xEEEE;
  const int tokX = 16 * ((nl >> 1) & 1) + 8 * (nl >> 3) + 2 * ((nl >> 2) & 1) + (nl & 1);
  u32x4 kxr[4];
  auto load_kx = [&](int s) {
#pragma unroll
    for (int i = 0; i < 4; ++i) {
      const int f = (w - 1) * 4 + i, tile = f / 6, u = f % 6;
      kxr[i] = __builtin_amdgcn_raw_buffer_load_b128(rws, (int)OFF_KX + ((s * 32 + h) * 32 + tokX + 4 * tile) * 384 + u * 64 + g * 16, 0, 0);
    }
  };
  if (w > 0) load_kx(0);
  const int row0 = 4 * (g & 1) + 2 * (g >> 1);
  const int qoff = L_QK + row0 * QROW + (48 * w + 3 * nl) * 2;
  const float cs = 0.07216878364870322f * 1.4426950408889634f;
  for (int s = 0; s < NSEG; ++s) {
    u32x8 ring[NRING];
#pragma unroll
    for (int i = 0; i < NRING; ++i) ring[i] = wlds(L, wl, i);
    if (!gather_mem(rws, s, w, lane, L, ctrl)) return;
    wg_barrier();
    u32x4 am[10];
#pragma unroll
    for (int T = 0; T < 10; ++T) am[T] = lds_r128(L, L_MEM + T * 1024 + lane * 16);
    f32x4 acc[6];
#pragma unroll
    for (int t = 0; t < 6; ++t) acc[t] = f32x4{0.f, 0.f, 0.f, 0.f};
    NOP_IN3(acc[0], acc[1], acc[2]);
    NOP_IN3(acc[3], acc[4], acc[5]);
    sfor<60>([&](auto F_) {
      constexpr int F = decltype(F_)::v;
      smfw<F>(acc[F % 6], am[F / 6], W, ring[f_idx(F) % NRING], idx);
      if constexpr (f_loc(F) == LOC_L && f_idx(F) + NRING < NLDS) ring[f_idx(F) % NRING] = wlds(L, wl, f_idx(F) + NRING);
    });
    if (w > 0) {
#pragma unroll
      for (int i = 0; i < 4; ++i) lds_w128(L, L_KX + ((w - 1) * 4 + i) * 1024 + lane * 16, kxr[i]);
    }
    NOP_OUT3(acc[0], acc[1], acc[2]);
    NOP_OUT3(acc[3], acc[4], acc[5]);
#pragma unroll
    for (int t = 0; t < 6; ++t) {
      const float a0 = swap_add32(acc[t][0], acc[t][2]);
      const float a1 = swap_add32(acc[t][1], acc[t][3]);
      const unsigned pk = pack2(a0, a1);
      const int o = qoff + (t >= 3 ? 8 * QROW : 0) + (t % 3) * 2;
      *(LAS u16*)(L + o) = (u16)(pk & 0xFFFFu);
      *(LAS u16*)(L + o + QROW) = (u16)(pk >> 16);
    }
    wg_barrier();
    if (w == 0) {
      if (s < NSEG - 1) {
        const int mrow = 4 * ((nl >> 1) & 1) + 2 * ((nl >> 2) & 1) + (nl & 1);
        f32x4 aX = {0.f, 0.f, 0.f, 0.f}, aY = aX, aM = aX;
        u32x4 qf[6], km[6], kX[6], kY[6];
#pragma unroll
        for (int u = 0; u < 6; ++u) {
          qf[u] = lds_r128(L, L_QK + (nl & 7) * QROW + u * 64 + g * 16);
          kX[u] = lds_r128(L, L_KX + u * 1024 + lane * 16);
          kY[u] = lds_r128(L, L_KX + (6 + u) * 1024 + lane * 16);
          km[u] = lds_r128(L, L_QK + (8 + mrow) * QROW + u * 64 + g * 16);
        }
        NOP_IN3(aX, aY, aM);
#pragma unroll
        for (int u = 0; u < 6; ++u) {
          MF_VV(aX, kX[u], qf[u]);
          MF_VV(aY, kY[u], qf[u]);
          MF_VV(aM, km[u], qf[u]);
        }
        NOP_OUT3(aX, aY, aM);
        if (g >= 2) aM = f32x4{-INFINITY, -INFINITY, -INFINITY, -INFINITY};
        float mx = fmaxf(fmaxf(fmaxf(aX[0], aX[1]), fmaxf(aX[2], aX[3])), fmaxf(fmaxf(aY[0], aY[1]), fmaxf(aY[2], aY[3])));
        mx = fmaxf(mx, fmaxf(fmaxf(aM[0], aM[1]), fmaxf(aM[2], aM[3])));
        mx = max_g(mx);
        float sum = 0.f;
#pragma unroll
        for (int r = 0; r < 4; ++r) {
          aX[r] = __builtin_amdgcn_exp2f((aX[r] - mx) * cs);
          aY[r] = __builtin_amdgcn_exp2f((aY[r] - mx) * cs);
          aM[r] = __builtin_amdgcn_exp2f((aM[r] - mx) * cs);
          sum += aX[r] + aY[r] + aM[r];
        }
        sum = sum_g(sum);
        const float inv = __builtin_amdgcn_rcpf(sum);
        u32x4 px = {nosent(pack2(aX[0] * inv, aX[1] * inv)), nosent(pack2(aX[2] * inv, aX[3] * inv)),
                    nosent(pack2(aY[0] * inv, aY[1] * inv)), nosent(pack2(aY[2] * inv, aY[3] * inv))};
        u32x2 pm = {nosent(pack2(aM[0] * inv, aM[1] * inv)), nosent(pack2(aM[2] * inv, aM[3] * inv))};
        const int pb = (int)OFF_PB + s * 20480 + h * 640;
        if (nl < 8) {
          __builtin_amdgcn_raw_buffer_store_b128(px, rws, pb + (g * 8 + nl) * 16, 0, AUX_SC1);
          if (g < 2) __builtin_amdgcn_raw_buffer_store_b64(pm, rws, pb + 512 + (g * 8 + nl) * 8, 0, AUX_SC1);
        }
      }
    } else {
      const int un = tid - 64, r = un / 24, cc = un % 24;
      const u32x4 kv = lds_r128(L, L_QK + (8 + r) * QROW + cc * 16);
      __builtin_amdgcn_raw_buffer_store_b128(kv, rws, (int)OFF_KM + (s * 32 + h) * 3072 + un * 16, 0, 0);
      if (s + 1 < NSEG) load_kx(s + 1);
    }
  }
}

DEV void v_role(const Params& p, int h, LAS unsigned char* L) {
  const int tid = threadIdx.x, lane = tid & 63, w = __builtin_amdgcn_readfirstlane(tid >> 6);
  const int nl = lane & 15, g = lane >> 4;
  const int wl = L_W + w * (NLDS * 2048) + lane * 16;
  unsigned* ctrl = (unsigned*)(p.ws + OFF_CTRL);
  WSet W;
  load_proj<true, 0, 3>(p.wv, h, w, lane, W, L, wl);
  load_wo<30>(p.wo, h, w, lane, W, L, wl);
  const rsrc_t rws = mkrsrc(p.ws, (unsigned)WS_END);
  const int idx = (nl < 8) ? 0x4444 : 0xEEEE;
  const int row0 = 4 * (g & 1) + 2 * (g >> 1);
  int voff[3];
#pragma unroll
  for (int t = 0; t < 3; ++t) {
    const int k = 48 * w + 3 * nl + t;
    voff[t] = L_QK + (k >> 6) * 1024 + (((k >> 4) & 3) * 16 + ((k >> 1) & 1) * 8 + row0) * 16 + ((k >> 2) & 3) * 4 + (k & 1) * 2;
  }
  for (int s = 0; s < NSEG; ++s) {
    u32x8 ring[NRING];
#pragma unroll
    for (int i = 0; i < NRING; ++i) ring[i] = wlds(L, wl, i);
    if (!gather_mem(rws, s, w, lane, L, ctrl)) return;
    wg_barrier();
    u32x4 am[10];
#pragma unroll
    for (int T = 0; T < 10; ++T) am[T] = lds_r128(L, L_MEM + T * 1024 + lane * 16);
    f32x4 acc[3];
#pragma unroll
    for (int t = 0; t < 3; ++t) acc[t] = f32x4{0.f, 0.f, 0.f, 0.f};
    NOP_IN3(acc[0], acc[1], acc[2]);
    sfor<30>([&](auto F_) {
      constexpr int F = decltype(F_)::v;
      smfw<F>(acc[F % 3], am[F / 3], W, ring[f_idx(F) % NRING], idx);
      if constexpr (f_loc(F) == LOC_L && f_idx(F) + NRING < NLDS) ring[f_idx(F) % NRING] = wlds(L, wl, f_idx(F) + NRING);
    });
    NOP_OUT3(acc[0], acc[1], acc[2]);
#pragma unroll
    for (int t = 0; t < 3; ++t) {
      const float a0 = swap_add32(acc[t][0], acc[t][2]);
      const float a1 = swap_add32(acc[t][1], acc[t][3]);
      const unsigned pk = pack2(a0, a1);
      *(LAS u16*)(L + voff[t]) = (u16)(pk & 0xFFFFu);
      *(LAS u16*)(L + voff[t] + 16) = (u16)(pk >> 16);
    }
    wg_barrier();
    u32x4 va[3];
#pragma unroll
    for (int T = 0; T < 3; ++T) va[T] = lds_r128(L, L_QK + T * 1024 + lane * 16);
    const int ob = (int)OFF_VOM + ((s * 40 + 10 * w + (g >> 1)) * 32 + h) * 256 + nl * 16 + (g & 1) * 8;
    f32x4 o0[5], o1[5];
#pragma unroll
    for (int i = 0; i < 5; ++i) { o0[i] = f32x4{0.f, 0.f, 0.f, 0.f}; o1[i] = o0[i]; }
    sfor<6>([&](auto P_) {
      constexpr int P = decltype(P_)::v;
      if constexpr (P < 5) {
        NOP_IN2(o0[P], o1[P]);
        sfor<6>([&](auto Q_) {
          constexpr int Q = decltype(Q_)::v, F = 30 + P * 6 + Q;
          smfw<F>((Q & 1) ? o1[P] : o0[P], va[Q / 2], W, ring[f_idx(F) % NRING], idx);
          if constexpr (f_loc(F) == LOC_L && f_idx(F) + NRING < NLDS) ring[f_idx(F) % NRING] = wlds(L, wl, f_idx(F) + NRING);
        });
      }
      if constexpr (P > 0) {
        if constexpr (P == 5) NOP_OUT2(o0[P - 1], o1[P - 1]); else NOP_IN2(o0[P - 1], o1[P - 1]);
        u32x2 o = {nosent(pack2(swap_add32(o0[P - 1][0], o1[P - 1][0]), swap_add32(o0[P - 1][1], o1[P - 1][1]))),
                   nosent(pack2(swap_add32(o0[P - 1][2], o1[P - 1][2]), swap_add32(o0[P - 1][3], o1[P - 1][3])))};
        __builtin_amdgcn_raw_buffer_store_b64(o, rws, ob + (P - 1) * 16384, 0, AUX_SC1);
      }
    });
  }
}

DEV void col_role(const Params& p, int j, LAS unsigned char* L) {
  const int tid = threadIdx.x, lane = tid & 63, w = __builtin_amdgcn_readfirstlane(tid >> 6);
  const int nl = lane & 15, g = lane >> 4;
  unsigned* ctrl = (unsigned*)(p.ws + OFF_CTRL);
  const rsrc_t rws = mkrsrc(p.ws, (unsigned)WS_END);
  const int idx = (nl < 8) ? 0x4444 : 0xEEEE;
  const int row0 = 4 * (g & 1) + 2 * (g >> 1);
  u32x4 vx[8];
  auto load_vox = [&](int s) {
#pragma unroll
    for (int i = 0; i < 8; ++i)
      vx[i] = __builtin_amdgcn_raw_buffer_load_b128(rws, (int)OFF_VOX + ((s * 40 + j) * 32 + 8 * w + i) * 1024 + lane * 16, 0, 0);
  };
  load_vox(0);
  for (int s = 0; s < NSEG - 1; ++s) {
    const int pb = (int)OFF_PB + s * 20480 + 8 * w * 640;
    const int px_off = pb + (g >> 1) * 640 + ((g & 1) * 16 + nl) * 16;
    const int pm_off = pb + 2 * g * 640 + 512 + nl * 8;
    const int vm_off = (int)OFF_VOM + ((s * 40 + j) * 32 + 8 * w) * 256 + lane * 16;
    u32x4 pa[4], vm0, vm1;
    u32x2 pm0, pm1;
    unsigned spins = 0;
    for (;;) {
      asm volatile("" ::: "memory");
#pragma unroll
      for (int q = 0; q < 4; ++q) pa[q] = __builtin_amdgcn_raw_buffer_load_b128(rws, px_off + q * 1280, 0, AUX_SC1);
      pm0 = __builtin_amdgcn_raw_buffer_load_b64(rws, pm_off, 0, AUX_SC1);
      pm1 = __builtin_amdgcn_raw_buffer_load_b64(rws, pm_off + 640, 0, AUX_SC1);
      vm0 = __builtin_amdgcn_raw_buffer_load_b128(rws, vm_off, 0, AUX_SC1);
      vm1 = __builtin_amdgcn_raw_buffer_load_b128(rws, vm_off + 1024, 0, AUX_SC1);
      bool ok = valid4(pa[0]) && valid4(pa[1]) && valid4(pa[2]) && valid4(pa[3]) && valid2(pm0) && valid2(pm1) && valid4(vm0) && valid4(vm1);
      if (__all(ok)) break;
      if (++spins > SPIN_CAP) { if (lane == 0) ctrl[0] = 2u; return; }
    }
    f32x4 a0 = {0.f, 0.f, 0.f, 0.f}, a1 = a0;
    a0 = smf(pa[0], cat8(vx[0], vx[1]), a0, idx);
    a1 = smf(pa[1], cat8(vx[2], vx[3]), a1, idx);
    a0 = smf(pa[2], cat8(vx[4], vx[5]), a0, idx);
    a1 = smf(pa[3], cat8(vx[6], vx[7]), a1, idx);
    a0 = smf(u32x4{pm0.x, pm0.y, pm1.x, pm1.y}, cat8(vm0, vm1), a0, idx);
    a0 += a1;
    const float r0 = swap_add32(a0[0], a0[2]);
    const float r1 = swap_add32(a0[1], a0[3]);
    LAS float* red = (LAS float*)(L + (s & 1) * 4096);
    red[(w * 8 + row0) * 18 + nl] = r0;
    red[(w * 8 + row0 + 1) * 18 + nl] = r1;
    wg_barrier();
    if (w == 0) {
      const int i = lane >> 3, hf = (lane >> 2) & 1, d = lane & 3;
      f32x2 sum = {0.f, 0.f};
#pragma unroll
      for (int ww = 0; ww < 4; ++ww) sum += *(LAS f32x2*)(red + (ww * 8 + i) * 18 + 4 * d + 2 * hf);
      const unsigned o = nosent(pack2(sum.x, sum.y));
      u32x4 o4;
      o4.x = o;
      o4.y = __builtin_amdgcn_update_dpp(0, o, 0x55, 0xf, 0xf, false);
      o4.z = __builtin_amdgcn_update_dpp(0, o, 0xAA, 0xf, 0xf, false);
      o4.w = __builtin_amdgcn_update_dpp(0, o, 0xFF, 0xf, 0xf, false);
      if (d == 0) __builtin_amdgcn_raw_buffer_store_b128(o4, rws, (int)OFF_MEMB + (s + 1) * 10240 + j * 256 + (hf * 8 + i) * 16, 0, AUX_SC1);
    }
    load_vox(s + 1);
  }
}

__global__ void __launch_bounds__(256, 1) k_chain(Params p) {
  extern __shared__ __attribute__((aligned(16))) unsigned char smem[];
  LAS unsigned char* L = (LAS unsigned char*)smem;
  const int b = blockIdx.x;
  if (b < 32) qk_role(p, b, L);
  else if (b < 64) v_role(p, b - 32, L);
  else col_role(p, b - 64, L);
}

DEV void proj_t(f32x4 (&acc)[2][3], rsrc_t rws, int hf_off, const WSetD& W, RingD (&ring)[NRING], LAS unsigned char* L, int wl, int nlds) {
#pragma unroll
  for (int mt = 0; mt < 2; ++mt)
#pragma unroll
    for (int t = 0; t < 3; ++t) acc[mt][t] = f32x4{0.f, 0.f, 0.f, 0.f};
  u32x4 hb[3][2][2];
  auto ldh = [&](int T, u32x4 (&d)[2][2]) {
#pragma unroll
    for (int mt = 0; mt < 2; ++mt)
#pragma unroll
      for (int z = 0; z < 2; ++z) d[mt][z] = __builtin_amdgcn_raw_buffer_load_b128(rws, hf_off + mt * 20480 + (2 * T + z) * 1024, 0, 0);
  };
  ldh(0, hb[0]); ldh(1, hb[1]);
  NOP_IN3(acc[0][0], acc[0][1], acc[0][2]);
  NOP_IN3(acc[1][0], acc[1][1], acc[1][2]);
  sfor<10>([&](auto T_) {
    constexpr int T = decltype(T_)::v;
    if constexpr (T + 2 < 10) ldh(T + 2, hb[(T + 2) % 3]);
    sfor<3>([&](auto t_) {
      constexpr int t = decltype(t_)::v, F = T * 3 + t;
      mfw<F, true, false>(acc[0][t], hb[T % 3][0][0], W, ring[f_idx(F) % NRING]);
      mfw<F, true, false>(acc[1][t], hb[T % 3][1][0], W, ring[f_idx(F) % NRING]);
      mfw<F, true, true>(acc[0][t], hb[T % 3][0][1], W, ring[f_idx(F) % NRING]);
      mfw<F, true, true>(acc[1][t], hb[T % 3][1][1], W, ring[f_idx(F) % NRING]);
      if constexpr (f_loc(F) == LOC_L) { if (f_idx(F) + NRING < nlds) ring[f_idx(F) % NRING] = wldsD(L, wl, f_idx(F) + NRING); }
    });
  });
  NOP_OUT3(acc[0][0], acc[0][1], acc[0][2]);
  NOP_OUT3(acc[1][0], acc[1][1], acc[1][2]);
}
#define RING_INIT() RingD ring[NRING]; _Pragma("unroll") for (int i = 0; i < NRING; ++i) ring[i] = wldsD(L, wl, i)
DEV void pack12(const f32x4 (&a)[3], u32x2 (&o)[3]) {
  o[0] = u32x2{pack2(a[0][0], a[1][0]), pack2(a[2][0], a[0][1])};
  o[1] = u32x2{pack2(a[1][1], a[2][1]), pack2(a[0][2], a[1][2])};
  o[2] = u32x2{pack2(a[2][2], a[0][3]), pack2(a[1][3], a[2][3])};
}

DEV void kx_role(const Params& p, int h, int s0, int s1, LAS unsigned char* L) {
  const int tid = threadIdx.x, lane = tid & 63, w = __builtin_amdgcn_readfirstlane(tid >> 6);
  const int nl = lane & 15, g = lane >> 4;
  const int wl = L_W + w * (NLDS * 2048) + lane * 16;
  WSetD W;
  load_proj<false, 0, 3>(p.wk, h, w, lane, W, L, wl);
  const rsrc_t rws = mkrsrc(p.ws, (unsigned)WS_END);
  for (int s = s0; s < s1; ++s) {
    f32x4 acc[2][3];
    RING_INIT();
    proj_t(acc, rws, (int)OFF_HF + s * 40960 + lane * 16, W, ring, L, wl, 7);
#pragma unroll
    for (int mt = 0; mt < 2; ++mt) {
      u32x2 o[3];
      pack12(acc[mt], o);
      const int ob = (int)OFF_KX + ((s * 32 + h) * 32 + mt * 16 + nl) * 384 + 96 * w + 24 * g;
#pragma unroll
      for (int i = 0; i < 3; ++i) __builtin_amdgcn_raw_buffer_store_b64(o[i], rws, ob + i * 8, 0, 0);
    }
  }
}

DEV void vox_role(const Params& p, int h, int s0, int s1, LAS unsigned char* L) {
  const int tid = threadIdx.x, lane = tid & 63, w = __builtin_amdgcn_readfirstlane(tid >> 6);
  const int nl = lane & 15, g = lane >> 4;
  const int wl = L_W + w * (NLDS * 2048) + lane * 16;
  WSetD W;
  load_proj<false, 0, 3>(p.wv, h, w, lane, W, L, wl);
  load_wo<30>(p.wo, h, w, lane, W, L, wl);
  const rsrc_t rws = mkrsrc(p.ws, (unsigned)WS_END);
  for (int s = s0; s < s1; ++s) {
    f32x4 acc[2][3];
    RING_INIT();
    proj_t(acc, rws, (int)OFF_HF + s * 40960 + lane * 16, W, ring, L, wl, NLDS);
    const int vb = L_MEM + (s & 1) * 12800;
#pragma unroll
    for (int mt = 0; mt < 2; ++mt) {
      u32x2 o[3];
      pack12(acc[mt], o);
#pragma unroll
      for (int i = 0; i < 3; ++i) *(LAS u32x2*)(L + vb + (mt * 16 + nl) * QROW + 96 * w + 24 * g + i * 8) = o[i];
    }
    wg_barrier();
    u32x4 va[2][6];
#pragma unroll
    for (int mt = 0; mt < 2; ++mt)
#pragma unroll
      for (int u = 0; u < 6; ++u) va[mt][u] = lds_r128(L, vb + (mt * 16 + nl) * QROW + u * 64 + g * 16);
    f32x4 o[2][2][2];
    sfor<6>([&](auto P_) {
      constexpr int P = decltype(P_)::v, B = P & 1;
      if constexpr (P < 5) {
#pragma unroll
        for (int i = 0; i < 4; ++i) o[B][i >> 1][i & 1] = f32x4{0.f, 0.f, 0.f, 0.f};
        NOP_IN2(o[B][0][0], o[B][0][1]);
        NOP_IN2(o[B][1][0], o[B][1][1]);
        sfor<6>([&](auto Q_) {
          constexpr int Q = decltype(Q_)::v, F = 30 + P * 6 + Q, T = Q / 2, tt = Q & 1;
          mfw<F, false, false>(o[B][tt][0], va[0][2 * T], W, ring[f_idx(F) % NRING]);
          mfw<F, false, false>(o[B][tt][1], va[1][2 * T], W, ring[f_idx(F) % NRING]);
          mfw<F, false, true>(o[B][tt][0], va[0][2 * T + 1], W, ring[f_idx(F) % NRING]);
          mfw<F, false, true>(o[B][tt][1], va[1][2 * T + 1], W, ring[f_idx(F) % NRING]);
          if constexpr (f_loc(F) == LOC_L && f_idx(F) + NRING < NLDS) ring[f_idx(F) % NRING] = wldsD(L, wl, f_idx(F) + NRING);
        });
      }
      if constexpr (P > 0) {
        constexpr int C = B ^ 1;
        if constexpr (P == 5) { NOP_OUT2(o[C][0][0], o[C][0][1]); NOP_OUT2(o[C][1][0], o[C][1][1]); }
        else { NOP_IN2(o[C][0][0], o[C][0][1]); NOP_IN2(o[C][1][0], o[C][1][1]); }
#pragma unroll
        for (int tt = 0; tt < 2; ++tt) {
          u32x4 ov = {pack2(o[C][tt][0][0], o[C][tt][0][1]), pack2(o[C][tt][0][2], o[C][tt][0][3]),
                      pack2(o[C][tt][1][0], o[C][tt][1][1]), pack2(o[C][tt][1][2], o[C][tt][1][3])};
          __builtin_amdgcn_raw_buffer_store_b128(ov, rws, (int)OFF_VOX + ((s * 40 + 10 * w + 2 * (P - 1) + tt) * 32 + h) * 1024 + lane * 16, 0, 0);
        }
      }
    });
  }
}

__global__ void __launch_bounds__(256, 1) k_x1(Params p) {
  extern __shared__ __attribute__((aligned(16))) unsigned char smem[];
  LAS unsigned char* L = (LAS unsigned char*)smem;
  const int b = blockIdx.x;
  if (b < 96) { const int h = b & 31, q = b >> 5; kx_role(p, h, (q * 64) / 3, ((q + 1) * 64) / 3, L); }
  else { const int bb = b - 96, h = bb & 31, q = bb >> 5; vox_role(p, h, (q * 64) / 5, ((q + 1) * 64) / 5, L); }
}

__global__ void __launch_bounds__(256, 1) k_px(Params p) {
  extern __shared__ __attribute__((aligned(16))) unsigned char smem[];
  LAS unsigned char* L = (LAS unsigned char*)smem;
  const int h = blockIdx.x & 31, s0 = (blockIdx.x >> 5) * 8;
  const int tid = threadIdx.x, lane = tid & 63, w = __builtin_amdgcn_readfirstlane(tid >> 6);
  const int nl = lane & 15, g = lane >> 4;
  const int wl = L_W + w * (NLDS * 2048) + lane * 16;
  WSetD W;
  load_proj<false, 0, 3>(p.wq, h, w, lane, W, L, wl);
  const rsrc_t rws = mkrsrc(p.ws, (unsigned)WS_END);
  const float cs = 0.07216878364870322f * 1.4426950408889634f;
  for (int s = s0; s < s0 + 8; ++s) {
    f32x4 acc[2][3];
    RING_INIT();
    proj_t(acc, rws, (int)OFF_HF + s * 40960 + lane * 16, W, ring, L, wl, 7);
    const int vb = L_MEM + (s & 1) * 12800;
#pragma unroll
    for (int mt = 0; mt < 2; ++mt) {
      u32x2 o[3];
      pack12(acc[mt], o);
#pragma unroll
      for (int i = 0; i < 3; ++i) *(LAS u32x2*)(L + vb + (mt * 16 + nl) * QROW + 96 * w + 24 * g + i * 8) = o[i];
    }
    wg_barrier();
    if (w < 2) {
      const int mt = w;
      f32x4 aX = {0.f, 0.f, 0.f, 0.f}, aY = aX, aM = aX;
      const int kxb = (int)OFF_KX + ((s * 32 + h) * 32 + nl) * 384 + g * 16;
      const int kmb = (int)OFF_KM + (s * 32 + h) * 3072 + (nl & 7) * 384 + g * 16;
#pragma unroll
      for (int u = 0; u < 6; ++u) {
        const u32x4 qf = lds_r128(L, vb + (mt * 16 + nl) * QROW + u * 64 + g * 16);
        const u32x4 kX = __builtin_amdgcn_raw_buffer_load_b128(rws, kxb + u * 64, 0, 0);
        const u32x4 kY = __builtin_amdgcn_raw_buffer_load_b128(rws, kxb + 16 * 384 + u * 64, 0, 0);
        const u32x4 km = __builtin_amdgcn_raw_buffer_load_b128(rws, kmb + u * 64, 0, 0);
        aX = mfma(kX, qf, aX);
        aY = mfma(kY, qf, aY);
        aM = mfma(km, qf, aM);
      }
      const int tok = mt * 16 + nl;
#pragma unroll
      for (int r = 0; r < 4; ++r) {
        if (4 * g + r > tok) aX[r] = -INFINITY;
        if (16 + 4 * g + r > tok) aY[r] = -INFINITY;
        if (g >= 2) aM[r] = -INFINITY;
      }
      float mx = fmaxf(fmaxf(fmaxf(aX[0], aX[1]), fmaxf(aX[2], aX[3])), fmaxf(fmaxf(aY[0], aY[1]), fmaxf(aY[2], aY[3])));
      mx = fmaxf(mx, fmaxf(fmaxf(aM[0], aM[1]), fmaxf(aM[2], aM[3])));
      mx = max_g(mx);
      float sum = 0.f;
#pragma unroll
      for (int r = 0; r < 4; ++r) {
        aX[r] = __builtin_amdgcn_exp2f((aX[r] - mx) * cs);
        aY[r] = __builtin_amdgcn_exp2f((aY[r] - mx) * cs);
        aM[r] = __builtin_amdgcn_exp2f((aM[r] - mx) * cs);
        sum += aX[r] + aY[r] + aM[r];
      }
      sum = sum_g(sum);
      const float inv = __builtin_amdgcn_rcpf(sum);
      u32x4 px = {pack2(aX[0] * inv, aX[1] * inv), pack2(aX[2] * inv, aX[3] * inv), pack2(aY[0] * inv, aY[1] * inv), pack2(aY[2] * inv, aY[3] * inv)};
      u32x2 pm = {pack2(aM[0] * inv, aM[1] * inv), pack2(aM[2] * inv, aM[3] * inv)};
      const int pb = (int)OFF_PX + (s * 2 + mt) * 40960;
      __builtin_amdgcn_raw_buffer_store_b128(px, rws, pb + h * 1024 + lane * 16, 0, 0);
      if (g < 2) __builtin_amdgcn_raw_buffer_store_b64(pm, rws, pb + (32 + (h >> 2)) * 1024 + ((h & 3) * 16 + nl) * 16 + g * 8, 0, 0);
    }
  }
}

__global__ void __launch_bounds__(256) k_xcol(Params p) {
  __shared__ __attribute__((aligned(16))) float red[4][32][164];
  const int s = blockIdx.x >> 2, q = blockIdx.x & 3;
  const int tid = threadIdx.x, lane = tid & 63, w = __builtin_amdgcn_readfirstlane(tid >> 6);
  const int nl = lane & 15, g = lane >> 4;
  const rsrc_t rws = mkrsrc(p.ws, (unsigned)WS_END);
  f32x4 acc[2][10];
#pragma unroll
  for (int mt = 0; mt < 2; ++mt)
#pragma unroll
    for (int t = 0; t < 10; ++t) acc[mt][t] = f32x4{0.f, 0.f, 0.f, 0.f};
#pragma unroll 2
  for (int kk = 0; kk < 10; ++kk) {
    const int step = kk < 8 ? 8 * w + kk : 32 + 2 * w + (kk - 8);
    u32x4 a[2], b[10];
#pragma unroll
    for (int mt = 0; mt < 2; ++mt) a[mt] = __builtin_amdgcn_raw_buffer_load_b128(rws, (int)OFF_PX + ((s * 2 + mt) * 40 + step) * 1024 + lane * 16, 0, 0);
#pragma unroll
    for (int t = 0; t < 10; ++t) {
      const int j = 10 * q + t;
      const int off = kk < 8 ? (int)OFF_VOX + ((s * 40 + j) * 32 + step) * 1024 : (int)OFF_VOM + ((s * 40 + j) * 32) * 256 + (step - 32) * 1024;
      b[t] = __builtin_amdgcn_raw_buffer_load_b128(rws, off + lane * 16, 0, 0);
    }
#pragma unroll
    for (int t = 0; t < 10; ++t)
#pragma unroll
      for (int mt = 0; mt < 2; ++mt) acc[mt][t] = mfma(a[mt], b[t], acc[mt][t]);
  }
#pragma unroll
  for (int mt = 0; mt < 2; ++mt)
#pragma unroll
    for (int t = 0; t < 10; ++t)
#pragma unroll
      for (int r = 0; r < 4; ++r) red[w][mt * 16 + 4 * g + r][10 * nl + t] = acc[mt][t][r];
  __syncthreads();
  const float* ga = (const float*)(p.ws + OFF_MOD) + 2 * D_;
  float* x1 = (float*)(p.ws + OFF_X1);
  for (int i = tid; i < 32 * 40; i += 256) {
    const int tok = i / 40, c4 = (i % 40) * 4;
    f32x4 v = *(const f32x4*)&red[0][tok][c4] + *(const f32x4*)&red[1][tok][c4] + *(const f32x4*)&red[2][tok][c4] + *(const f32x4*)&red[3][tok][c4];
    const int col = 160 * q + c4, row = s * 32 + tok;
    const u32x2 xv = *(const u32x2*)(p.x + (size_t)row * D_ + col);
    const f32x4 gv = *(const f32x4*)(ga + col);
    f32x4 o = {bflo(xv.x) + gv.x * v.x, bfhi(xv.x) + gv.y * v.y, bflo(xv.y) + gv.z * v.z, bfhi(xv.y) + gv.w * v.w};
    *(f32x4*)(x1 + (size_t)row * D_ + col) = o;
  }
}

DEV float gelu_tanh(float x) {
  const float u = 0.7978845608028654f * (x + 0.044715f * x * x * x);
  return x * __builtin_amdgcn_rcpf(1.f + __builtin_amdgcn_exp2f(-2.885390081777927f * u));
}

__global__ void __launch_bounds__(256) k_ffn1(Params p) {
  const int tb = blockIdx.x >> 3, fb = blockIdx.x & 7;
  const int tid = threadIdx.x, lane = tid & 63, w = __builtin_amdgcn_readfirstlane(tid >> 6);
  const int nl = lane & 15, g = lane >> 4;
  const rsrc_t rws = mkrsrc(p.ws, (unsigned)WS_END);
  const rsrc_t rg = mkrsrc(p.wg, D_ * DF * 2), ru = mkrsrc(p.wu, D_ * DF * 2);
  const int col = fb * 192 + 48 * w + 3 * nl;
  const unsigned sh = (col & 1) * 16;
  f32x4 ag[4][3], au[4][3];
#pragma unroll
  for (int mt = 0; mt < 4; ++mt)
#pragma unroll
    for (int t = 0; t < 3; ++t) { ag[mt][t] = f32x4{0.f, 0.f, 0.f, 0.f}; au[mt][t] = ag[mt][t]; }
#pragma unroll 2
  for (int u = 0; u < 20; ++u) {
    const int voff = ((32 * u + 8 * g) * DF + (col & ~1)) * 2;
    u32x4 fg[3], fu[3], hb[4];
    load3<false, DF>(rg, voff, sh, 0, fg);
    load3<false, DF>(ru, voff, sh, 0, fu);
#pragma unroll
    for (int mt = 0; mt < 4; ++mt) hb[mt] = __builtin_amdgcn_raw_buffer_load_b128(rws, (int)OFF_H2 + (tb * 4 + mt) * 20480 + u * 1024 + lane * 16, 0, 0);
#pragma unroll
    for (int t = 0; t < 3; ++t)
#pragma unroll
      for (int mt = 0; mt < 4; ++mt) { ag[mt][t] = mfma(fg[t], hb[mt], ag[mt][t]); au[mt][t] = mfma(fu[t], hb[mt], au[mt][t]); }
  }
  const int f0 = fb * 192 + 48 * w + 12 * g;
#pragma unroll
  for (int mt = 0; mt < 4; ++mt) {
    f32x4 a[3];
#pragma unroll
    for (int t = 0; t < 3; ++t)
#pragma unroll
      for (int r = 0; r < 4; ++r) a[t][r] = gelu_tanh(ag[mt][t][r]) * au[mt][t][r];
    u32x2 o[3];
    pack12(a, o);
#pragma unroll
    for (int i = 0; i < 3; ++i) {
      const int f = f0 + 4 * i;
      __builtin_amdgcn_raw_buffer_store_b64(o[i], rws, (int)OFF_ACT + (tb * 4 + mt) * 49152 + (f >> 5) * 1024 + (((f >> 3) & 3) * 16 + nl) * 16 + (f & 7) * 2, 0, 0);
    }
  }
}

__global__ void __launch_bounds__(256) k_ffn2(Params p) {
  __shared__ __attribute__((aligned(16))) float red[4][32][164];
  const int s = blockIdx.x >> 2, q = blockIdx.x & 3;
  const int tid = threadIdx.x, lane = tid & 63, w = __builtin_amdgcn_readfirstlane(tid >> 6);
  const int nl = lane & 15, g = lane >> 4;
  const rsrc_t rws = mkrsrc(p.ws, (unsigned)WS_END);
  const rsrc_t rd = mkrsrc(p.wd, D_ * DF * 2);
  f32x4 acc[2][10];
#pragma unroll
  for (int mt = 0; mt < 2; ++mt)
#pragma unroll
    for (int t = 0; t < 10; ++t) acc[mt][t] = f32x4{0.f, 0.f, 0.f, 0.f};
#pragma unroll 2
  for (int kk = 0; kk < 12; ++kk) {
    const int u = 12 * w + kk;
    u32x4 a[2], b[10];
    load10<D_>(rd, ((32 * u + 8 * g) * D_ + 160 * q + 10 * nl) * 2, 0, b);
#pragma unroll
    for (int mt = 0; mt < 2; ++mt) a[mt] = __builtin_amdgcn_raw_buffer_load_b128(rws, (int)OFF_ACT + (s * 2 + mt) * 49152 + u * 1024 + lane * 16, 0, 0);
#pragma unroll
    for (int t = 0; t < 10; ++t)
#pragma unroll
      for (int mt = 0; mt < 2; ++mt) acc[mt][t] = mfma(a[mt], b[t], acc[mt][t]);
  }
#pragma unroll
  for (int mt = 0; mt < 2; ++mt)
#pragma unroll
    for (int t = 0; t < 10; ++t)
#pragma unroll
      for (int r = 0; r < 4; ++r) red[w][mt * 16 + 4 * g + r][10 * nl + t] = acc[mt][t][r];
  __syncthreads();
  const float* gb = (const float*)(p.ws + OFF_MOD) + 5 * D_;
  const float* x1 = (const float*)(p.ws + OFF_X1);
  for (int i = tid; i < 32 * 40; i += 256) {
    const int tok = i / 40, c4 = (i % 40) * 4;
    f32x4 v = *(const f32x4*)&red[0][tok][c4] + *(const f32x4*)&red[1][tok][c4] + *(const f32x4*)&red[2][tok][c4] + *(const f32x4*)&red[3][tok][c4];
    const int col = 160 * q + c4, row = s * 32 + tok;
    const f32x4 xv = *(const f32x4*)(x1 + (size_t)row * D_ + col);
    const f32x4 gv = *(const f32x4*)(gb + col);
    u32x2 o = {pack2(xv.x + gv.x * v.x, xv.y + gv.y * v.y), pack2(xv.z + gv.z * v.z, xv.w + gv.w * v.w)};
    *(u32x2*)(p.out + (size_t)row * D_ + col) = o;
  }
}

extern "C" void kernel_launch(void* const* d_in, const int* in_sizes, int n_in, void* d_out, int out_size, void* d_ws, size_t ws_size,
                              hipStream_t stream) {
  Params p;
  p.x = (const u16*)d_in[0]; p.mem0 = (const u16*)d_in[1]; p.wq = (const u16*)d_in[2]; p.wk = (const u16*)d_in[3];
  p.wv = (const u16*)d_in[4]; p.wo = (const u16*)d_in[5]; p.wg = (const u16*)d_in[8]; p.wu = (const u16*)d_in[9];
  p.wd = (const u16*)d_in[10]; p.c = (const u16*)d_in[11]; p.wmod = (const u16*)d_in[12];
  p.out = (u16*)d_out; p.ws = (unsigned char*)d_ws;
  constexpr int LDSB = L_END;
  (void)hipFuncSetAttribute((const void*)k_chain, hipFuncAttributeMaxDynamicSharedMemorySize, LDSB);
  (void)hipFuncSetAttribute((const void*)k_x1, hipFuncAttributeMaxDynamicSharedMemorySize, LDSB);
  (void)hipFuncSetAttribute((const void*)k_px, hipFuncAttributeMaxDynamicSharedMemorySize, LDSB);
  k_pro<<<512, 256, 0, stream>>>(p);
  k_ln<false><<<128, 256, 0, stream>>>(p);
  k_x1<<<256, 256, LDSB, stream>>>(p);
  k_chain<<<104, 256, LDSB, stream>>>(p);
  k_px<<<256, 256, LDSB, stream>>>(p);
  k_xcol<<<256, 256, 0, stream>>>(p);
  k_ln<true><<<128, 256, 0, stream>>>(p);
  k_ffn1<<<256, 256, 0, stream>>>(p);
  k_ffn2<<<256, 256, 0, stream>>>(p);
}
```

```cpp
#include <hip/hip_runtime.h>
#include <hip/hip_bf16.h>
#include <stdint.h>
#include <utility>

typedef __bf16 bf16x2 __attribute__((ext_vector_type(2)));
typedef float f32x2 __attribute__((ext_vector_type(2)));
typedef float f32x4 __attribute__((ext_vector_type(4)));
typedef unsigned u32x2 __attribute__((ext_vector_type(2)));
typedef unsigned u32x4 __attribute__((ext_vector_type(4)));
typedef unsigned u32x8 __attribute__((ext_vector_type(8)));
typedef unsigned short u16;
typedef __amdgpu_buffer_rsrc_t rsrc_t;
#define LAS __attribute__((address_space(3)))
#define DEV __device__ __forceinline__

constexpr int S_ = 2048, D_ = 640, DH = 192, HD = 6144, NSEG = 64, DF = 1536;
constexpr int AUX_SC1 = 16;
constexpr unsigned SENT = 0xFFFFFFFFu;
constexpr unsigned SPIN_CAP = 1u << 20;

constexpr size_t OFF_CTRL = 0;
constexpr size_t OFF_PROG = 4096;
constexpr int PG_KX = 0, PG_KM = 128, PG_PX = 256, PG_VOX = 320, PG_QX = 448, PG_MOD = 624, PG_H = 688, PG_FILL = 752, PG_FILL2 = 792, PG_GO = 840, PG_N = 856;
constexpr size_t OFF_MOD = 114688;
static_assert(OFF_PROG + PG_N * 128 <= OFF_MOD, "ctrl");
constexpr size_t OFF_HF = 131072;
constexpr size_t SZ_HF = (size_t)S_ * D_ * 2;
constexpr size_t OFF_KX = OFF_HF + SZ_HF;
constexpr size_t SZ_KX = (size_t)S_ * HD * 2;
constexpr size_t OFF_QX = OFF_KX + SZ_KX;
constexpr size_t OFF_VOX = OFF_QX + SZ_KX;
constexpr size_t SZ_VOX = (size_t)NSEG * 40 * 32 * 1024;
constexpr size_t OFF_KM = OFF_VOX + SZ_VOX;
constexpr size_t SZ_KM = (size_t)NSEG * 32 * 6144;
constexpr size_t OFF_PX = OFF_KM + SZ_KM;
constexpr size_t SZ_PX = (size_t)NSEG * 2 * 40 * 1024;
constexpr size_t WS_END = OFF_PX + SZ_PX;
constexpr size_t OFF_COMM = WS_END;
constexpr unsigned COMM_BYTES = 64u << 20;
static_assert(OFF_COMM + COMM_BYTES + (1u << 20) <= (256u << 20), "ws");
constexpr int CB_P = 2, CB_VOM = 6, CB_NEAR = 46, CB_N = 63, FR_X1 = 0, FR_ACT = 40;
constexpr int CTRL_TICKET = 64, CTRL_SEL = 512;

struct Params {
  const u16 *x, *mem0, *wq, *wk, *wv, *wo, *wg, *wu, *wd, *c, *wmod;
  u16* out;
  unsigned char* ws;
};

DEV rsrc_t mkrsrc(const void* p, unsigned bytes) { return __builtin_amdgcn_make_buffer_rsrc((void*)p, 0, (int)bytes, 0x00020000); }
DEV unsigned char* comm_base(unsigned char* ws) {
  return (unsigned char*)(((uintptr_t)(ws + OFF_COMM) + ((1u << 20) - 1)) & ~(uintptr_t)((1u << 20) - 1));
}
DEV unsigned long long load_selmask(unsigned char* ws, int lane) {
  const unsigned long long raw = __builtin_amdgcn_ballot_w64(((const unsigned*)(ws + OFF_CTRL))[CTRL_SEL + lane] != 0u);
  const unsigned long long pat = (((uintptr_t)comm_base(ws) >> 20) & 1) ? 0x6666666666666666ull : 0xCCCCCCCCCCCCCCCCull;
  const int diff = __builtin_popcountll(raw ^ pat);
  return diff <= 22 ? pat : (diff >= 42 ? ~pat : raw);
}
DEV int step_base(unsigned long long mask, int s) { return (s << 20) + (int)((mask >> s) & 1ull) * 8192; }
DEV int frag_off(int sb, int f) { return (sb ^ 8192) + (f >> 3) * 16384 + (f & 7) * 1024; }
DEV int blk_off(int sb, int k) { return k < CB_NEAR ? sb + k * 16384 : (sb ^ 8192) + (k - CB_NEAR) * 16384; }
DEV unsigned xcc_id() { return (unsigned)__builtin_amdgcn_s_getreg(20 | (3 << 11)) & 0xfu; }
template <class T> DEV void st128(T v, rsrc_t rs, int voff, int soff, int aux) {
  if (aux == AUX_SC1) __builtin_amdgcn_raw_buffer_store_b128(v, rs, voff, soff, AUX_SC1);
  else __builtin_amdgcn_raw_buffer_store_b128(v, rs, voff, soff, 0);
  asm volatile("s_nop 1" :: "v"(v) : "memory");
}
DEV float bf2f(unsigned b) { return __builtin_bit_cast(float, b << 16); }
DEV float bflo(unsigned d) { return __builtin_bit_cast(float, d << 16); }
DEV float bfhi(unsigned d) { return __builtin_bit_cast(float, d & 0xFFFF0000u); }
DEV unsigned pack2(float a, float b) { f32x2 v = {a, b}; return __builtin_bit_cast(unsigned, __builtin_convertvector(v, bf16x2)); }
DEV unsigned nosent(unsigned d) { return d > 0xFFFFFFFEu ? 0xFFFFFFFEu : d; }
DEV float rbf(float a) { return bflo(pack2(a, 0.f) & 0xFFFFu); }
DEV u32x4 lo4(u32x8 v) { return __builtin_shufflevector(v, v, 0, 1, 2, 3); }
DEV u32x4 hi4(u32x8 v) { return __builtin_shufflevector(v, v, 4, 5, 6, 7); }
DEV u32x8 cat8(u32x4 a, u32x4 b) { return __builtin_shufflevector(a, b, 0, 1, 2, 3, 4, 5, 6, 7); }
DEV void wg_barrier() { asm volatile("s_waitcnt lgkmcnt(0)\n\ts_barrier" ::: "memory"); }
DEV unsigned umax3(unsigned a, unsigned b, unsigned c) { const unsigned m = a > b ? a : b; return m > c ? m : c; }
DEV unsigned umax4(unsigned m, u32x4 v) { return umax3(umax3(m, v.x, v.y), v.z, v.w); }
DEV bool all_valid(unsigned m) { return __builtin_amdgcn_ballot_w64(m == SENT) == 0ull; }
DEV float swap_add32(float a, float b) {
  auto r = __builtin_amdgcn_permlane32_swap(__builtin_bit_cast(unsigned, a), __builtin_bit_cast(unsigned, b), false, false);
  return __builtin_bit_cast(float, (unsigned)r[0]) + __builtin_bit_cast(float, (unsigned)r[1]);
}
DEV float max_g(float x) {
  unsigned u = __builtin_bit_cast(unsigned, x);
  auto r = __builtin_amdgcn_permlane32_swap(u, u, false, false);
  x = fmaxf(__builtin_bit_cast(float, (unsigned)r[0]), __builtin_bit_cast(float, (unsigned)r[1]));
  u = __builtin_bit_cast(unsigned, x);
  auto q = __builtin_amdgcn_permlane16_swap(u, u, false, false);
  return fmaxf(__builtin_bit_cast(float, (unsigned)q[0]), __builtin_bit_cast(float, (unsigned)q[1]));
}
DEV float sum_g(float x) {
  unsigned u = __builtin_bit_cast(unsigned, x);
  auto r = __builtin_amdgcn_permlane32_swap(u, u, false, false);
  x = __builtin_bit_cast(float, (unsigned)r[0]) + __builtin_bit_cast(float, (unsigned)r[1]);
  u = __builtin_bit_cast(unsigned, x);
  auto q = __builtin_amdgcn_permlane16_swap(u, u, false, false);
  return __builtin_bit_cast(float, (unsigned)q[0]) + __builtin_bit_cast(float, (unsigned)q[1]);
}
DEV u32x4 lds_r128(LAS unsigned char* L, int off) { return *(LAS u32x4*)(L + off); }
DEV void lds_w128(LAS unsigned char* L, int off, u32x4 v) { *(LAS u32x4*)(L + off) = v; }

template <bool PERM> DEV int lane_row(int gb) { return PERM ? (gb >> 1) + 80 * (gb & 1) : 8 * gb; }
template <bool PERM> DEV int base_row(int u) { return PERM ? 160 * (u / 5) + 2 * (u % 5) : 32 * u; }

struct Raw10 { u32x4 a[8]; unsigned b[8]; };
template <int LDW>
DEV void load10_issue(rsrc_t rs, int voff, int u, Raw10& r) {
#pragma unroll
  for (int jj = 0; jj < 8; ++jj) {
    r.a[jj] = __builtin_amdgcn_raw_buffer_load_b128(rs, voff, (32 * u + jj) * LDW * 2, 0);
    r.b[jj] = __builtin_amdgcn_raw_buffer_load_b32(rs, voff + 16, (32 * u + jj) * LDW * 2, 0);
  }
}
DEV void load10_finish(const Raw10& r, u32x4 (&f)[10]) {
#pragma unroll
  for (int t = 0; t < 10; ++t)
#pragma unroll
    for (int q = 0; q < 4; ++q) {
      const unsigned hi = t < 8 ? r.a[2 * q + 1][(t >> 1) & 3] : r.b[2 * q + 1];
      const unsigned lo = t < 8 ? r.a[2 * q][(t >> 1) & 3] : r.b[2 * q];
      f[t][q] = __builtin_amdgcn_perm(hi, lo, (t & 1) ? 0x07060302u : 0x05040100u);
    }
}
DEV float mod_cols(const Params& p, int cb, LAS float* red) {
  const int tid = threadIdx.x;
  const int ct = tid & 7, ks = tid >> 3;
  const int col = cb * 64 + ct * 8;
  float acc[8];
#pragma unroll
  for (int i = 0; i < 8; ++i) acc[i] = 0.f;
  u32x4 w[20];
  u32x2 cw[5];
#pragma unroll
  for (int k = 0; k < 5; ++k) cw[k] = *(const u32x2*)(p.c + ks * 20 + 4 * k);
#pragma unroll
  for (int k = 0; k < 20; ++k) w[k] = *(const u32x4*)(p.wmod + (size_t)(ks * 20 + k) * 3840 + col);
#pragma unroll
  for (int k = 0; k < 20; ++k) {
    const unsigned cd = cw[k >> 2][(k >> 1) & 1];
    const float cv = (k & 1) ? bfhi(cd) : bflo(cd);
    const float sv = rbf(cv / (1.f + __expf(-cv)));
#pragma unroll
    for (int q = 0; q < 4; ++q) { acc[2 * q] += sv * bflo(w[k][q]); acc[2 * q + 1] += sv * bfhi(w[k][q]); }
  }
#pragma unroll
  for (int i = 0; i < 8; ++i) red[ks * 65 + ct * 8 + i] = acc[i];
  wg_barrier();
  float sm = 0.f;
  if (tid < 64) {
#pragma unroll
    for (int k = 0; k < 32; ++k) sm += red[k * 65 + tid];
  }
  return rbf(sm);
}

__global__ void __launch_bounds__(256) k_init(Params p) {
  const int tid = threadIdx.x, b = blockIdx.x, w = __builtin_amdgcn_readfirstlane(tid >> 6);
  __shared__ float red[32 * 65];
  if (b > 64) {
    const int col = (b - 65) * 16 + (tid & 1) * 8, k0 = (tid >> 1) * 5;
    u32x4 wv[5];
    unsigned cv[5];
#pragma unroll
    for (int k = 0; k < 5; ++k) { wv[k] = *(const u32x4*)(p.wmod + (size_t)(k0 + k) * 3840 + col); cv[k] = p.c[k0 + k]; }
    float acc[8];
#pragma unroll
    for (int i = 0; i < 8; ++i) acc[i] = 0.f;
#pragma unroll
    for (int k = 0; k < 5; ++k) {
      const float c = bf2f(cv[k]);
      const float sv = rbf(c / (1.f + __expf(-c)));
#pragma unroll
      for (int q = 0; q < 4; ++q) { acc[2 * q] += sv * bflo(wv[k][q]); acc[2 * q + 1] += sv * bfhi(wv[k][q]); }
    }
#pragma unroll
    for (int i = 0; i < 8; ++i) red[((tid & 1) * 8 + i) * 129 + (tid >> 1)] = acc[i];
    __syncthreads();
    float sm = 0.f;
#pragma unroll
    for (int k = 0; k < 8; ++k) sm += red[(tid >> 4) * 129 + (tid & 15) + 16 * k];
#pragma unroll
    for (int o = 1; o < 16; o <<= 1) sm += __shfl_xor(sm, o);
    if ((tid & 15) == 0) ((float*)(p.ws + OFF_MOD))[(b - 65) * 16 + (tid >> 4)] = rbf(sm);
    return;
  }
  if (b == 64) {
    ((unsigned*)(p.ws + OFF_CTRL))[tid] = 0u;
    ((unsigned*)(p.ws + OFF_CTRL))[tid + 256] = 0u;
    for (int i = tid; i < PG_N; i += 256) *(unsigned*)(p.ws + OFF_PROG + (size_t)i * 128) = 0u;
    return;
  }
  const int s = b;
  const rsrc_t rc = mkrsrc(comm_base(p.ws), COMM_BYTES);
  if (w != 0) return;
  const int po = (s << 20) + (s & 31) * 16384 + (s & 1) * 4096 + ((s * 5) & 31) * 128 + (tid & 31) * 4;
  unsigned b0 = ~0u, b1 = ~0u, acc = 0;
#pragma unroll 1
  for (int k = 0; k < 2; ++k) {
    unsigned long long t0, t1, t2;
    __builtin_amdgcn_raw_buffer_store_b32(acc, rc, po + (tid >> 5) * 8192, 0, AUX_SC1);
    asm volatile("s_waitcnt vmcnt(0)" ::: "memory");
    asm volatile("s_memtime %0\n\ts_waitcnt lgkmcnt(0)" : "=s"(t0) :: "memory");
    acc = __builtin_amdgcn_raw_buffer_load_b32(rc, po, 0, AUX_SC1);
    asm volatile("s_waitcnt vmcnt(0)" : "+v"(acc) :: "memory");
    asm volatile("s_memtime %0\n\ts_waitcnt lgkmcnt(0)" : "=s"(t1) :: "memory");
    unsigned acc2 = __builtin_amdgcn_raw_buffer_load_b32(rc, po + 8192, 0, AUX_SC1);
    asm volatile("s_waitcnt vmcnt(0)" : "+v"(acc2) :: "memory");
    asm volatile("s_memtime %0\n\ts_waitcnt lgkmcnt(0)" : "=s"(t2) :: "memory");
    b0 = min(b0, (unsigned)(t1 - t0));
    b1 = min(b1, (unsigned)(t2 - t1));
  }
  if (tid == 0) ((unsigned*)(p.ws + OFF_CTRL))[CTRL_SEL + s] = (b1 < b0 ? 1u : 0u) ^ (xcc_id() >= 4 ? 1u : 0u);
}

template <int I> struct IC { static constexpr int v = I; };
template <class F, int... Is> DEV void sfor_impl(F&& f, std::integer_sequence<int, Is...>) { (f(IC<Is>{}), ...); }
template <int N, class F> DEV void sfor(F&& f) { sfor_impl(f, std::make_integer_sequence<int, N>{}); }

constexpr int NAG = 32, NVG = 12, NLDS = 16;
constexpr int WL_BYTES = 4 * NLDS * 2048;
enum { LOC_A = 0, LOC_V = 1, LOC_L = 2 };
constexpr bool f_lds(int f, int P = 0) { return P == 2 ? (f >= 6 && f % 3 == 1) : ((f & 3) == 3 || f == 33); }
constexpr int f_nl(int f, int P = 0) { int n = 0; for (int i = 0; i < f; ++i) n += f_lds(i, P) ? 1 : 0; return n; }
constexpr int f_loc(int f, int P = 0) { return f_lds(f, P) ? LOC_L : ((f - f_nl(f, P)) < NAG ? LOC_A : LOC_V); }
constexpr int f_idx(int f, int P = 0) { return f_lds(f, P) ? f_nl(f, P) : ((f - f_nl(f, P)) < NAG ? (f - f_nl(f, P)) : (f - f_nl(f, P)) - NAG); }
static_assert(f_nl(60) == NLDS && f_nl(60, 2) == 18, "lds frags");
#define ALL_AGPRS \
  "a0", "a1", "a2", "a3", "a4", "a5", "a6", "a7", "a8", "a9", "a10", "a11", "a12", "a13", "a14", "a15", \
  "a16", "a17", "a18", "a19", "a20", "a21", "a22", "a23", "a24", "a25", "a26", "a27", "a28", "a29", "a30", "a31", \
  "a32", "a33", "a34", "a35", "a36", "a37", "a38", "a39", "a40", "a41", "a42", "a43", "a44", "a45", "a46", "a47", \
  "a48", "a49", "a50", "a51", "a52", "a53", "a54", "a55", "a56", "a57", "a58", "a59", "a60", "a61", "a62", "a63", \
  "a64", "a65", "a66", "a67", "a68", "a69", "a70", "a71", "a72", "a73", "a74", "a75", "a76", "a77", "a78", "a79", \
  "a80", "a81", "a82", "a83", "a84", "a85", "a86", "a87", "a88", "a89", "a90", "a91", "a92", "a93", "a94", "a95", \
  "a96", "a97", "a98", "a99", "a100", "a101", "a102", "a103", "a104", "a105", "a106", "a107", "a108", "a109", "a110", "a111", \
  "a112", "a113", "a114", "a115", "a116", "a117", "a118", "a119", "a120", "a121", "a122", "a123", "a124", "a125", "a126", "a127", \
  "a128", "a129", "a130", "a131", "a132", "a133", "a134", "a135", "a136", "a137", "a138", "a139", "a140", "a141", "a142", "a143", \
  "a144", "a145", "a146", "a147", "a148", "a149", "a150", "a151", "a152", "a153", "a154", "a155", "a156", "a157", "a158", "a159", \
  "a160", "a161", "a162", "a163", "a164", "a165", "a166", "a167", "a168", "a169", "a170", "a171", "a172", "a173", "a174", "a175", \
  "a176", "a177", "a178", "a179", "a180", "a181", "a182", "a183", "a184", "a185", "a186", "a187", "a188", "a189", "a190", "a191", \
  "a192", "a193", "a194", "a195", "a196", "a197", "a198", "a199", "a200", "a201", "a202", "a203", "a204", "a205", "a206", "a207", \
  "a208", "a209", "a210", "a211", "a212", "a213", "a214", "a215", "a216", "a217", "a218", "a219", "a220", "a221", "a222", "a223", \
  "a224", "a225", "a226", "a227", "a228", "a229", "a230", "a231", "a232", "a233", "a234", "a235", "a236", "a237", "a238", "a239", \
  "a240", "a241", "a242", "a243", "a244", "a245", "a246", "a247", "a248", "a249", "a250", "a251", "a252", "a253", "a254", "a255"
struct WSet { static constexpr int POL = 0; u32x8 v[NVG]; };
template <int P> struct WSetP { static constexpr int POL = P; u32x4 v[60 - NAG - f_nl(60, P)][2]; };
DEV void claim_agprs() { asm volatile("" ::: ALL_AGPRS); }
template <int R> DEV void agpr_put4(u32x4 x) {
  asm volatile("v_accvgpr_write_b32 a[%c4], %0\n\tv_accvgpr_write_b32 a[%c4+1], %1\n\tv_accvgpr_write_b32 a[%c4+2], %2\n\tv_accvgpr_write_b32 a[%c4+3], %3"
               :: "v"(x.x), "v"(x.y), "v"(x.z), "v"(x.w), "i"(R) : ALL_AGPRS);
}
typedef short s16x4 __attribute__((ext_vector_type(4)));
template <int R4 = 384>
DEV u32x4 tr_frag(LAS unsigned char* L, int off) {
  const u32x2 a = __builtin_bit_cast(u32x2, __builtin_amdgcn_ds_read_tr16_b64_v4i16((LAS s16x4*)(L + off)));
  const u32x2 b = __builtin_bit_cast(u32x2, __builtin_amdgcn_ds_read_tr16_b64_v4i16((LAS s16x4*)(L + off + R4)));
  return u32x4{a.x, a.y, b.x, b.y};
}
template <int R, int OFF, int R4 = 384> DEV void tr_to_agpr(unsigned addr) {
  asm volatile("ds_read_b64_tr_b16 a[%c1:%c1+1], %0 offset:%c2\n\tds_read_b64_tr_b16 a[%c1+2:%c1+3], %0 offset:%c3"
               :: "v"(addr), "i"(R), "i"(OFF), "i"(OFF + R4) : "memory", ALL_AGPRS);
}
template <int F> DEV void wput(WSet& W, LAS unsigned char* L, int wl, u32x4 x, u32x4 y) {
  if constexpr (f_loc(F) == LOC_L) { lds_w128(L, wl + f_idx(F) * 2048, x); lds_w128(L, wl + f_idx(F) * 2048 + 1024, y); }
  else if constexpr (f_loc(F) == LOC_A) { agpr_put4<f_idx(F) * 8>(x); agpr_put4<f_idx(F) * 8 + 4>(y); }
  else { W.v[f_idx(F)] = cat8(x, y); asm volatile("" : "+v"(W.v[f_idx(F)])); }
}
template <int F, int P> DEV void wput(WSetP<P>& W, LAS unsigned char* L, int wl, u32x4 x, u32x4 y) {
  constexpr int I = f_idx(F, P);
  if constexpr (f_loc(F, P) == LOC_L) { lds_w128(L, wl + I * 2048, x); lds_w128(L, wl + I * 2048 + 1024, y); }
  else if constexpr (f_loc(F, P) == LOC_A) { agpr_put4<I * 8>(x); agpr_put4<I * 8 + 4>(y); }
  else { W.v[I][0] = x; W.v[I][1] = y; asm volatile("" : "+v"(W.v[I][0]), "+v"(W.v[I][1])); }
}
struct RingD { u32x4 h[2]; };

#define SMF_A(c, a, R, i) asm volatile("v_smfmac_f32_16x16x64_bf16 %0, %1, a[%c3:%c3+7], %2" : "+v"(c) : "v"(a), "v"(i), "i"(R) : ALL_AGPRS)
#define SMF_V(c, a, b, i) asm volatile("v_smfmac_f32_16x16x64_bf16 %0, %1, %2, %3" : "+v"(c) : "v"(a), "v"(b), "v"(i) : ALL_AGPRS)
template <int F> DEV void smfw(f32x4& c, const u32x4& a, const WSet& W, const u32x8& lb, int idx) {
  if constexpr (f_loc(F) == LOC_A) SMF_A(c, a, f_idx(F) * 8, idx);
  else if constexpr (f_loc(F) == LOC_V) SMF_V(c, a, W.v[f_idx(F)], idx);
  else SMF_V(c, a, lb, idx);
}
#define MF_AV(c, R, b) asm volatile("v_mfma_f32_16x16x32_bf16 %0, a[%c2:%c2+3], %1, %0" : "+v"(c) : "v"(b), "i"(R) : ALL_AGPRS)
#define MF_VA(c, a, R) asm volatile("v_mfma_f32_16x16x32_bf16 %0, %1, a[%c2:%c2+3], %0" : "+v"(c) : "v"(a), "i"(R) : ALL_AGPRS)
#define MF_VV(c, a, b) asm volatile("v_mfma_f32_16x16x32_bf16 %0, %1, %2, %0" : "+v"(c) : "v"(a), "v"(b) : ALL_AGPRS)
#define MF0_AV(c, R, b) asm volatile("v_mfma_f32_16x16x32_bf16 %0, a[%c2:%c2+3], %1, 0" : "=&v"(c) : "v"(b), "i"(R) : ALL_AGPRS)
#define MF0_VA(c, a, R) asm volatile("v_mfma_f32_16x16x32_bf16 %0, %1, a[%c2:%c2+3], 0" : "=&v"(c) : "v"(a), "i"(R) : ALL_AGPRS)
#define MF0_VV(c, a, b) asm volatile("v_mfma_f32_16x16x32_bf16 %0, %1, %2, 0" : "=&v"(c) : "v"(a), "v"(b) : ALL_AGPRS)
template <int R, bool FIRST> DEV void mfa(f32x4& c, const u32x4& x) { if constexpr (FIRST) MF0_AV(c, R, x); else MF_AV(c, R, x); }
DEV void mfv(f32x4& c, const u32x4& a, const u32x4& b) { MF_VV(c, a, b); }
template <bool FIRST> DEV void mfv2(f32x4& c, const u32x4& a, const u32x4& b) { if constexpr (FIRST) MF0_VV(c, a, b); else MF_VV(c, a, b); }
DEV void pin4(u32x4& a) { asm volatile("" : "+v"(a)); }
DEV f32x4 zero4() { float z; asm volatile("v_mov_b32 %0, 0" : "=v"(z)); return f32x4{z, z, z, z}; }
template <int F, bool WA, bool HI, bool FIRST = false, int P> DEV void mfw(f32x4& c, const u32x4& x, const WSetP<P>& W, const RingD& lb) {
  if constexpr (f_loc(F, P) == LOC_A) {
    constexpr int R = f_idx(F, P) * 8 + (HI ? 4 : 0);
    if constexpr (FIRST) { if constexpr (WA) MF0_AV(c, R, x); else MF0_VA(c, x, R); }
    else { if constexpr (WA) MF_AV(c, R, x); else MF_VA(c, x, R); }
  } else {
    const u32x4& wv = f_loc(F, P) == LOC_V ? W.v[f_loc(F, P) == LOC_V ? f_idx(F, P) : 0][HI] : lb.h[HI];
    if constexpr (FIRST) { if constexpr (WA) MF0_VV(c, wv, x); else MF0_VV(c, x, wv); }
    else { if constexpr (WA) MF_VV(c, wv, x); else MF_VV(c, x, wv); }
  }
}
#define NOP_IN3(a, b, c) asm volatile("s_nop 1" : "+v"(a), "+v"(b), "+v"(c))
#define NOP_OUT3(a, b, c) asm volatile("s_nop 7" : "+v"(a), "+v"(b), "+v"(c))

template <int MODE> constexpr int fmap(int T, int t) {
  return MODE == 0 ? T * 3 + t
       : MODE == 4 ? T * 6 + t
       : MODE == 5 ? T * 6 + 3 + t
       : MODE == 3 ? (T < 2 ? T * 3 + t : 6 + t * 8 + (T - 2))
                   : (T < 2 ? T * 6 + (MODE == 2 ? 3 : 0) + t : 12 + ((MODE == 2 ? 3 : 0) + t) * 8 + (T - 2));
}
DEV int chunk_of(int T, int w) { return T < 8 ? ((2 * w + T) & 7) : T; }
DEV int rot_chunk(int T, int h) { const int c = T + ((h >> 2) * 5) / 4; return c >= 10 ? c - 10 : c; }
template <int MODE, int MODE2, int WDEPTH, class WS, class Hook>
DEV void load_proj(const u16* Wm, const u16* Wm2, int h, int w, int lane, WS& W, LAS unsigned char* L, int wl, int stg, Hook&& hook) {
  constexpr bool PERM = MODE >= 1 && MODE <= 3;
  constexpr int NS = MODE2 >= 0 ? 40 : 20;
  const rsrc_t rs = mkrsrc(Wm, D_ * HD * 2), rs2 = mkrsrc(Wm2, D_ * HD * 2);
  const int nl = lane & 15, gb = lane >> 4;
  int voff[3];
#pragma unroll
  for (int i = 0; i < 3; ++i) {
    const int un = lane + 64 * i, r = un / 6, cu = un % 6;
    voff[i] = ((lane_row<PERM>(r >> 3) + (PERM ? 10 : 1) * (r & 7)) * HD + h * DH + 48 * w) * 2 + cu * 16;
  }
  int sw[3];
#pragma unroll
  for (int i = 0; i < 3; ++i) { const int un = lane + 64 * i; sw[i] = stg + w * 3584 + un * 16 + (un / 48) * 128; }
  const int sr = stg + w * 3584 + gb * 896 + (nl >> 2) * 96 + (nl & 3) * 8;
  const unsigned sra = (unsigned)(uintptr_t)(L + sr);
  u32x4 raw[WDEPTH][3];
  auto issue = [&](int st, u32x4 (&r)[3]) {
    const int u = st % 20;
    const int c = PERM ? chunk_of(u >> 1, w) : rot_chunk(u >> 1, h);
    const int so = base_row<PERM>(2 * c + (u & 1)) * HD * 2;
#pragma unroll
    for (int i = 0; i < 3; ++i) r[i] = __builtin_amdgcn_raw_buffer_load_b128(st < 20 ? rs : rs2, voff[i], so, 0);
  };
#pragma unroll
  for (int u = 0; u < WDEPTH - 1; ++u) issue(u, raw[u]);
  u32x4 a[3];
  sfor<NS>([&](auto U_) {
    constexpr int st = decltype(U_)::v, u = st % 20, M = st < 20 ? MODE : MODE2;
    if constexpr (st + WDEPTH - 1 < NS) issue(st + WDEPTH - 1, raw[(st + WDEPTH - 1) % WDEPTH]);
    if constexpr (st + WDEPTH - 4 == NS) hook();
    asm volatile("" ::: "memory");
#pragma unroll
    for (int i = 0; i < 3; ++i) lds_w128(L, sw[i], raw[st % WDEPTH][i]);
    asm volatile("" ::: "memory");
    sfor<3>([&](auto t_) {
      constexpr int t = decltype(t_)::v, F = fmap<M>(u / 2, t);
      if constexpr (f_loc(F, WS::POL) == LOC_A) tr_to_agpr<f_idx(F, WS::POL) * 8 + (u & 1) * 4, t * 32>(sra);
      else {
        const u32x4 x = tr_frag(L, sr + t * 32);
        if constexpr ((u & 1) == 0) a[t] = x; else wput<F>(W, L, wl, a[t], x);
      }
    });
    asm volatile("" ::: "memory");
  });
  asm volatile("s_waitcnt lgkmcnt(0)" ::: "memory");
}
template <int MODE, int FB, class WS>
DEV void load_vo(const u16* Wv, const u16* Wm, int h, int w, int lane, WS& W, LAS unsigned char* L, int wl, int stg) {
  const rsrc_t rs = mkrsrc(Wm, D_ * HD * 2);
  const int nl = lane & 15, gb = lane >> 4;
  const int voff = ((h * DH + 8 * gb) * D_ + 160 * w + 10 * nl) * 2;
  Raw10 r[3];
  load_proj<MODE, -1, 10>(Wv, Wv, h, w, lane, W, L, wl, stg, [&]() {
    load10_issue<D_>(rs, voff, 0, r[0]);
    load10_issue<D_>(rs, voff, 1, r[1]);
  });
  u32x4 a[10];
  sfor<6>([&](auto U_) {
    constexpr int u = decltype(U_)::v, T = u / 2;
    if constexpr (u + 2 < 6 && (u & 1) == 0) load10_issue<D_>(rs, voff, u + 2, r[(u + 2) % 3]);
    asm volatile("" ::: "memory");
    if constexpr ((u & 1) == 0) {
      load10_finish(r[u % 3], a);
#pragma unroll
      for (int t = 0; t < 10; ++t) asm volatile("" : "+v"(a[t]));
      if constexpr (u + 3 < 6) load10_issue<D_>(rs, voff, u + 3, r[(u + 3) % 3]);
    } else {
      u32x4 b[10];
      load10_finish(r[u % 3], b);
      sfor<10>([&](auto t_) { constexpr int t = decltype(t_)::v; wput<(FB < 0 ? t * 6 + 3 + T : FB + (t / 2) * 6 + T * 2 + (t & 1))>(W, L, wl, a[t], b[t]); });
    }
  });
}
constexpr int NRING = 3;

constexpr int L_W = 0;
constexpr int L_MEM = WL_BYTES;
constexpr int L_QK = L_MEM + 10240;
constexpr int L_PS = L_QK + 4 * 3584;
constexpr int L_FLAG = L_PS + 6144;
constexpr int L_END = L_FLAG + 64;
constexpr int QROW = 400;
#define PIN1(a) asm volatile("" : "+v"(a))
#define PIN2(a, b) asm volatile("" : "+v"(a), "+v"(b))
#define MEMFENCE() asm volatile("" ::: "memory")
#define MF16_VV(c, a, b) asm volatile("v_mfma_f32_16x16x16_bf16 %0, %1, %2, %0" : "+v"(c) : "v"(a), "v"(b) : ALL_AGPRS)

DEV void publish(rsrc_t rws, int word, unsigned val, int lane) {
  asm volatile("s_waitcnt vmcnt(0)" ::: "memory");
  if (lane == 0) __builtin_amdgcn_raw_buffer_store_b32(val, rws, (int)OFF_PROG + word * 128, 0, AUX_SC1);
}
template <int SLEEP = 2>
DEV bool wait_prog(rsrc_t rws, int woff, unsigned want, unsigned& seen, unsigned* ctrl) {
  if (seen >= want) return true;
  unsigned v, spins = 0;
  for (;;) {
    MEMFENCE();
    v = __builtin_amdgcn_raw_buffer_load_b32(rws, woff, 0, AUX_SC1);
    if (__builtin_amdgcn_ballot_w64(v < want) == 0ull) break;
    if (++spins > SPIN_CAP) { ctrl[0] = 9u; return false; }
    __builtin_amdgcn_s_sleep(SLEEP);
  }
  seen = want;
#pragma unroll
  for (unsigned d = 16; d >= 1; d >>= 1)
    if (__builtin_amdgcn_ballot_w64(v < seen + d) == 0ull) seen += d;
  MEMFENCE();
  return true;
}
struct Pend { int off; unsigned val; bool on; };
DEV void pend_flag(rsrc_t rws, Pend& pd, int lane) {
  if (lane == 0) __builtin_amdgcn_raw_buffer_store_b32(pd.val, rws, pd.off, 0, AUX_SC1);
  pd.on = false;
}
template <int N> DEV void pend_after_loads(rsrc_t rws, Pend& pd, int lane) {
  MEMFENCE();
  if (pd.on) { asm volatile("s_waitcnt vmcnt(%0)" :: "i"(N) : "memory"); pend_flag(rws, pd, lane); }
}
DEV bool wait_prog_pub(rsrc_t rws, int woff, unsigned want, unsigned& seen, unsigned* ctrl, Pend& pd, int lane) {
  if (seen >= want) return true;
  if (pd.on) {
    MEMFENCE();
    const unsigned v = __builtin_amdgcn_raw_buffer_load_b32(rws, woff, 0, AUX_SC1);
    asm volatile("s_waitcnt vmcnt(0)" ::: "memory");
    pend_flag(rws, pd, lane);
    if (__builtin_amdgcn_ballot_w64(v < want) == 0ull) {
      seen = want;
#pragma unroll
      for (unsigned d = 16; d >= 1; d >>= 1)
        if (__builtin_amdgcn_ballot_w64(v < seen + d) == 0ull) seen += d;
      MEMFENCE();
      return true;
    }
  }
  return wait_prog<1>(rws, woff, want, seen, ctrl);
}
DEV int pg_off(int word) { return (int)OFF_PROG + word * 128; }

DEV unsigned rt32() { return (unsigned)__builtin_amdgcn_s_memrealtime(); }
DEV void barrier_time(unsigned long long& t) { asm volatile("s_waitcnt lgkmcnt(0)\n\ts_barrier\n\ts_memrealtime %0" : "=s"(t) :: "memory"); }
DEV void time_wait(unsigned long long& t) { asm volatile("s_waitcnt lgkmcnt(0)" : "+s"(t) :: "memory"); }
struct MemAddr { int pr[4]; int c8; };
DEV void mem_addr(MemAddr& a, int w, int moff) {
#pragma unroll
  for (int k = 0; k < 4; ++k) { a.pr[k] = L_MEM + ((w + k) & 3) * 2048 + moff; PIN1(a.pr[k]); }
  a.c8 = L_MEM + 8192 + moff;
  PIN1(a.c8);
}
template <class Hook>
DEV bool gather_own(rsrc_t rws, int base, int w, int lane, int moff, const MemAddr& ma, LAS unsigned char* L, unsigned* ctrl, u32x4& v0, u32x4& v1,
                    Hook&& idle) {
  unsigned spins = 0;
  for (;;) {
    MEMFENCE();
    v0 = __builtin_amdgcn_raw_buffer_load_b128(rws, moff, base + w * 2048, AUX_SC1);
    v1 = __builtin_amdgcn_raw_buffer_load_b128(rws, moff + 1024, base + w * 2048, AUX_SC1);
    const u32x2 v2 = __builtin_amdgcn_raw_buffer_load_b64(rws, lane * 8, base + 16384 + w * 512, AUX_SC1);
    lds_w128(L, ma.pr[0], v0);
    lds_w128(L, ma.pr[0] + 1024, v1);
    *(LAS u32x2*)(L + L_MEM + 8192 + w * 512 + lane * 8) = v2;
    if (all_valid(umax3(umax4(umax4(0u, v0), v1), v2.x, v2.y))) break;
    idle();
    if (++spins > SPIN_CAP) { ctrl[0] = 1u; return false; }
  }
  asm volatile("s_waitcnt vmcnt(0)" ::: "memory");
  return true;
}
template <int K> DEV void read_pair(u32x4 (&am)[10], const MemAddr& ma, LAS unsigned char* L) {
  const int a = K < 4 ? ma.pr[K < 4 ? K : 0] : ma.c8;
  am[2 * K] = lds_r128(L, a);
  am[2 * K + 1] = lds_r128(L, a + 1024);
  MEMFENCE();
}
struct Ring8 { u32x4 h[NRING][2]; };
template <int I, int HALF> DEV void ring_ld(Ring8& r, LAS unsigned char* L, int wl) {
  if constexpr (I < NLDS) { r.h[I % NRING][HALF] = lds_r128(L, wl + I * 2048 + HALF * 1024); MEMFENCE(); }
}
template <int F> DEV void smfr(f32x4& c, const u32x4& a, const WSet& W, const Ring8& r, int idx) {
  constexpr int S = f_idx(F) % NRING;
  smfw<F>(c, a, W, cat8(r.h[S][0], r.h[S][1]), idx);
}
template <int F, int FMIN> DEV void ring_step(Ring8& r, LAS unsigned char* L, int wl) {
  if constexpr (F >= FMIN && f_lds(F)) ring_ld<f_idx(F) + NRING, 0>(r, L, wl);
  if constexpr (F - 1 >= FMIN && f_lds(F - 1)) ring_ld<f_idx(F - 1) + NRING, 1>(r, L, wl);
}
struct Epi { float a0, a1, b0, b1; unsigned pk; };
DEV void epi1(Epi& e, f32x4& acc) {
  PIN1(acc);
  const u32x4 au = __builtin_bit_cast(u32x4, acc);
  auto r = __builtin_amdgcn_permlane32_swap(au.x, au.z, false, false);
  auto q = __builtin_amdgcn_permlane32_swap(au.y, au.w, false, false);
  e.a0 = __builtin_bit_cast(float, (unsigned)r[0]); e.b0 = __builtin_bit_cast(float, (unsigned)r[1]);
  e.a1 = __builtin_bit_cast(float, (unsigned)q[0]); e.b1 = __builtin_bit_cast(float, (unsigned)q[1]);
  PIN2(e.a0, e.b0); PIN2(e.a1, e.b1);
}
DEV void epi2(Epi& e) { e.pk = pack2(e.a0 + e.b0, e.a1 + e.b1); PIN1(e.pk); }
DEV void epi3(const Epi& e, LAS unsigned char* L, int off, int rowstep) {
  *(LAS u16*)(L + off) = (u16)(e.pk & 0xFFFFu);
  *(LAS u16*)(L + off + rowstep) = (u16)(e.pk >> 16);
  MEMFENCE();
}
constexpr int T_VM = -3;
constexpr int T_P = 5;
constexpr int T_MEM = 78;
constexpr int T_FLAG = 166;
DEV void wait_rt(unsigned t) { while (t - rt32() - 1u < 512u) { } }
template <int NF>
DEV bool flags_max(rsrc_t rc, int base, int lane, unsigned tref, unsigned& tmax) {
  const int off = base + (lane & (NF - 1)) * 128;
  unsigned v, spins = 0;
  bool ok;
  do {
    MEMFENCE();
    v = __builtin_amdgcn_raw_buffer_load_b32(rc, off, 0, AUX_SC1);
    ok = all_valid(v);
  } while (!ok && ++spins < SPIN_CAP);
  int d = (int)(v - tref);
#pragma unroll
  for (int o = 1; o < NF; o <<= 1) { const int e = __shfl_xor(d, o); d = d > e ? d : e; }
  tmax = tref + (unsigned)__builtin_amdgcn_readfirstlane(d);
  return ok;
}
DEV bool flags_max8_scalar(const unsigned char* ptr, unsigned tref, unsigned& tmax) {
  unsigned f0, f1, f2, f3, f4, f5, f6, f7;
  asm volatile("s_load_dword %0, %8, 0x0\n\ts_load_dword %1, %8, 0x80\n\ts_load_dword %2, %8, 0x100\n\ts_load_dword %3, %8, 0x180\n\t"
               "s_load_dword %4, %8, 0x200\n\ts_load_dword %5, %8, 0x280\n\ts_load_dword %6, %8, 0x300\n\ts_load_dword %7, %8, 0x380\n\t"
               "s_waitcnt lgkmcnt(0)"
               : "=&s"(f0), "=&s"(f1), "=&s"(f2), "=&s"(f3), "=&s"(f4), "=&s"(f5), "=&s"(f6), "=&s"(f7) : "s"(ptr) : "memory");
  if (umax3(umax3(umax3(f0, f1, f2), f3, f4), f5, f6 > f7 ? f6 : f7) == SENT) return false;
  const unsigned d = umax3(umax3(umax3(f0 - tref, f1 - tref, f2 - tref), f3 - tref, f4 - tref), f5 - tref, umax3(f6 - tref, f7 - tref, 0u));
  tmax = tref + d;
  return (int)(tmax - rt32()) < 400;
}
DEV bool flags_max16(rsrc_t rc, const unsigned char* cbp, int off, int lane, unsigned tref, unsigned& tmax) {
  unsigned f[16];
  asm volatile("s_load_dword %0, %16, 0x0\n\ts_load_dword %1, %16, 0x80\n\ts_load_dword %2, %16, 0x100\n\ts_load_dword %3, %16, 0x180\n\ts_load_dword %4, %16, 0x200\n\ts_load_dword %5, %16, 0x280\n\ts_load_dword %6, %16, 0x300\n\ts_load_dword %7, %16, 0x380\n\ts_load_dword %8, %16, 0x400\n\ts_load_dword %9, %16, 0x480\n\ts_load_dword %10, %16, 0x500\n\ts_load_dword %11, %16, 0x580\n\ts_load_dword %12, %16, 0x600\n\ts_load_dword %13, %16, 0x680\n\ts_load_dword %14, %16, 0x700\n\ts_load_dword %15, %16, 0x780\n\ts_waitcnt lgkmcnt(0)"
               : "=&s"(f[0]), "=&s"(f[1]), "=&s"(f[2]), "=&s"(f[3]), "=&s"(f[4]), "=&s"(f[5]), "=&s"(f[6]), "=&s"(f[7]), "=&s"(f[8]), "=&s"(f[9]), "=&s"(f[10]), "=&s"(f[11]), "=&s"(f[12]), "=&s"(f[13]), "=&s"(f[14]), "=&s"(f[15])
               : "s"(cbp + off) : "memory");
  unsigned m = 0, d = 0;
#pragma unroll
  for (int i = 0; i < 16; ++i) { m = m > f[i] ? m : f[i]; d = d > f[i] - tref ? d : f[i] - tref; }
  tmax = tref + d;
  if (m != SENT && (int)(tmax - rt32()) < 400) return true;
  return flags_max<16>(rc, off, lane, tref, tmax);
}
DEV void sched_put(LAS unsigned char* L, int k, int tag, unsigned t) { *(volatile LAS u32x2*)(L + L_FLAG + k * 8) = u32x2{(unsigned)tag, t}; }
DEV bool sched_get(LAS unsigned char* L, int tag, unsigned& t) {
  for (unsigned spins = 0; spins < (SPIN_CAP << 4); ++spins) {
    const u32x4 v = *(volatile LAS u32x4*)(L + L_FLAG);
    if (__builtin_amdgcn_readfirstlane(v.x) == (unsigned)tag && __builtin_amdgcn_readfirstlane(v.z) == (unsigned)tag) {
      const unsigned a = __builtin_amdgcn_readfirstlane(v.y), b = __builtin_amdgcn_readfirstlane(v.w);
      t = (int)(a - b) > 0 ? a : b;
      return true;
    }
  }
  return false;
}
constexpr int TSF_OFF = 16384 + 2048;
DEV void add_ror8(f32x4& x) {
  float a = x[0], b = x[1], c = x[2], d = x[3];
  asm volatile("s_nop 1\n\tv_add_f32_dpp %0, %0, %0 row_ror:8 row_mask:0xf bank_mask:0xf\n\tv_add_f32_dpp %1, %1, %1 row_ror:8 row_mask:0xf bank_mask:0xf\n\t"
               "v_add_f32_dpp %2, %2, %2 row_ror:8 row_mask:0xf bank_mask:0xf\n\tv_add_f32_dpp %3, %3, %3 row_ror:8 row_mask:0xf bank_mask:0xf"
               : "+v"(a), "+v"(b), "+v"(c), "+v"(d));
  x = f32x4{a, b, c, d};
}
DEV void add_ror8_old(f32x4& x) {
  PIN1(x);
  float a = x[0], b = x[1], c = x[2], d = x[3];
  asm volatile("v_add_f32_dpp %0, %0, %0 row_ror:8 row_mask:0xf bank_mask:0xf\n\tv_add_f32_dpp %1, %1, %1 row_ror:8 row_mask:0xf bank_mask:0xf\n\t"
               "v_add_f32_dpp %2, %2, %2 row_ror:8 row_mask:0xf bank_mask:0xf\n\tv_add_f32_dpp %3, %3, %3 row_ror:8 row_mask:0xf bank_mask:0xf"
               : "+v"(a), "+v"(b), "+v"(c), "+v"(d));
  x = f32x4{a, b, c, d};
}
#define RED8(OP, x)                                                                                                   \
  asm volatile("s_nop 1\n\t" OP " %0, %0, %0 quad_perm:[1,0,3,2] row_mask:0xf bank_mask:0xf\n\ts_nop 1\n\t"               \
               OP " %0, %0, %0 quad_perm:[2,3,0,1] row_mask:0xf bank_mask:0xf\n\ts_nop 1\n\t"                             \
               OP " %0, %0, %0 row_half_mirror row_mask:0xf bank_mask:0xf" : "+v"(x))
DEV float max3f(float a, float b, float c) { float r; asm("v_max3_f32 %0, %1, %2, %3" : "=v"(r) : "v"(a), "v"(b), "v"(c)); return r; }

DEV void publish_wg(rsrc_t rws, int word, unsigned val) {
  asm volatile("s_waitcnt vmcnt(0)" ::: "memory");
  wg_barrier();
  if (threadIdx.x == 0) __builtin_amdgcn_raw_buffer_store_b32(val, rws, (int)OFF_PROG + word * 128, 0, AUX_SC1);
}
template <int SLEEP = 8>
DEV bool wait_words(rsrc_t rws, int first, int n, int lane, unsigned* ctrl) {
  unsigned seen = 0;
  return wait_prog<SLEEP>(rws, pg_off(first + (lane < n ? lane : 0)), 1u, seen, ctrl);
}
DEV bool wait_startup(rsrc_t rws, unsigned* ctrl) { return wait_words<40>(rws, PG_GO, 1, 0, ctrl); }
DEV float ldmod(rsrc_t rws, int i) { return __builtin_bit_cast(float, __builtin_amdgcn_raw_buffer_load_b32(rws, i * 4, (int)OFF_MOD, AUX_SC1)); }

constexpr int NEARLY = 10;
DEV void fill_blk(rsrc_t rc, int off, int tid) {
  const u32x4 sv = {SENT, SENT, SENT, SENT};
  st128(sv, rc, tid * 16, off, AUX_SC1);
  st128(sv, rc, tid * 16 + 4096, off, AUX_SC1);
}
DEV void task_fill_early(const Params& p, int id) {
  const int tid = threadIdx.x;
  const rsrc_t rws = mkrsrc(p.ws, (unsigned)WS_END);
  const rsrc_t rc = mkrsrc(comm_base(p.ws), COMM_BYTES);
  const unsigned long long selm = load_selmask(p.ws, tid & 63);
  if (id == 39) {
    for (int un = tid; un < 1024; un += 256) {
      u32x4 v = {SENT, SENT, SENT, SENT};
      if (un < 640) {
        const int j = un >> 4, half = (un >> 2) & 1, i = 4 * ((un >> 3) & 1) + (un & 3);
        const u16* src = p.mem0 + i * D_ + 160 * (j / 10) + j % 10 + 20 * half;
#pragma unroll
        for (int d = 0; d < 4; ++d) v[d] = nosent((unsigned)src[40 * d] | ((unsigned)src[40 * d + 10] << 16));
      }
      st128(v, rc, (un & 511) * 16 + (un >> 9) * 16384, step_base(selm, 0), AUX_SC1);
    }
  }
  for (int k = 2 + id; k < NEARLY * CB_N; k += 40) fill_blk(rc, blk_off(step_base(selm, k / CB_N), k % CB_N), tid);
  publish_wg(rws, PG_FILL + id, 1u);
}
DEV void seen_update(unsigned v, unsigned long long lanes, unsigned& seen) {
#pragma unroll
  for (unsigned d = 8; d >= 1; d >>= 1)
    if ((__builtin_amdgcn_ballot_w64(v < seen + d) & lanes) == 0ull) seen += d;
}
constexpr unsigned long long LANES_FILL = (1ull << 40) - 1ull, LANES_PROD = ~LANES_FILL;
DEV bool wait_filled(rsrc_t rws, int s, unsigned& seen, int lane, unsigned* ctrl) {
  if (s < NEARLY || s >= NSEG) return true;
  return wait_prog<1>(rws, pg_off(PG_FILL2 + (lane < 40 ? lane : 0)), (unsigned)(((s - NEARLY) >> 1) + 1), seen, ctrl);
}

DEV void task_pull(const Params& p, int x, int r) {
  const int tid = threadIdx.x;
  if (tid >= 240) return;
  const int hh = tid / 30, rr = (tid % 30) / 3, ln = tid % 3;
  const unsigned char* q = (const unsigned char*)p.wq + (size_t)(r * 40 + rr) * (HD * 2) + (x + 4 * hh) * 384 + ln * 128;
  unsigned a = 0;
#pragma unroll
  for (int k = 0; k < 4; ++k) a ^= *(const unsigned*)(q + (size_t)k * 10 * HD * 2);
  asm volatile("" :: "v"(a));
}
DEV bool task_mod_ln(const Params& p, int c, LAS unsigned char* L) {
  const int tid = threadIdx.x, lane = tid & 63;
  const rsrc_t rws = mkrsrc(p.ws, (unsigned)WS_END);
  const int row16 = tid >> 4, l16 = tid & 15;
  u32x4 xr[2][5];
#pragma unroll
  for (int t = 0; t < 2; ++t)
#pragma unroll
    for (int q = 0; q < 5; ++q) xr[t][q] = *(const u32x4*)(p.x + (size_t)(c * 32 + t * 16 + row16) * D_ + (l16 + 16 * q) * 8);
  f32x4 shv[5][2], scv[5][2];
#pragma unroll
  for (int q = 0; q < 5; ++q)
#pragma unroll
    for (int z = 0; z < 2; ++z) {
      shv[q][z] = __builtin_bit_cast(f32x4, __builtin_amdgcn_raw_buffer_load_b128(rws, (l16 + 16 * q) * 32 + z * 16, (int)OFF_MOD, 0));
      scv[q][z] = __builtin_bit_cast(f32x4, __builtin_amdgcn_raw_buffer_load_b128(rws, (l16 + 16 * q) * 32 + z * 16, (int)OFF_MOD + D_ * 4, 0));
    }
  MEMFENCE();
  float mean[2], rstd[2];
#pragma unroll
  for (int t = 0; t < 2; ++t) {
    float sm = 0.f;
#pragma unroll
    for (int q = 0; q < 5; ++q)
#pragma unroll
      for (int i = 0; i < 4; ++i) sm += bflo(xr[t][q][i]) + bfhi(xr[t][q][i]);
#pragma unroll
    for (int o = 1; o < 16; o <<= 1) sm += __shfl_xor(sm, o);
    mean[t] = sm * (1.f / D_);
    float ss = 0.f;
#pragma unroll
    for (int q = 0; q < 5; ++q)
#pragma unroll
      for (int i = 0; i < 4; ++i) { const float d0 = bflo(xr[t][q][i]) - mean[t], d1 = bfhi(xr[t][q][i]) - mean[t]; ss += d0 * d0 + d1 * d1; }
#pragma unroll
    for (int o = 1; o < 16; o <<= 1) ss += __shfl_xor(ss, o);
    rstd[t] = 1.f / sqrtf(ss * (1.f / D_) + 1e-5f);
  }
#pragma unroll
  for (int q = 0; q < 5; ++q) {
    const int unit = l16 + 16 * q;
    const f32x4 (&sh)[2] = shv[q];
    const f32x4 (&sc)[2] = scv[q];
#pragma unroll
    for (int t = 0; t < 2; ++t) {
      u32x4 r;
#pragma unroll
      for (int i = 0; i < 4; ++i) {
        const float a = (bflo(xr[t][q][i]) - mean[t]) * rstd[t] * (1.f + sc[i >> 1][(2 * i) & 3]) + sh[i >> 1][(2 * i) & 3];
        const float b = (bfhi(xr[t][q][i]) - mean[t]) * rstd[t] * (1.f + sc[i >> 1][(2 * i + 1) & 3]) + sh[i >> 1][(2 * i + 1) & 3];
        r[i] = pack2(a, b);
      }
      st128(r, rws, (unit >> 2) * 1024 + ((unit & 3) * 16 + row16) * 16, (int)OFF_HF + (c * 2 + t) * 20480, AUX_SC1);
    }
  }
  publish_wg(rws, PG_H + c, 1u);
  if (c >= 24) {
    const float v = mod_cols(p, c - 4, (LAS float*)L);
    if (tid < 64) {
      __builtin_amdgcn_raw_buffer_store_b32(__builtin_bit_cast(unsigned, v), rws, ((c - 4) * 64 + tid) * 4, (int)OFF_MOD, AUX_SC1);
      publish(rws, PG_MOD + c - 24, 1u, lane);
    }
    wg_barrier();
  }
  return true;
}
DEV bool wait_h(rsrc_t rws, int lane, unsigned* ctrl) {
  if (!wait_words(rws, PG_H, 64, lane, ctrl)) return false;
  __builtin_amdgcn_fence(__ATOMIC_ACQUIRE, "agent");
  return true;
}

DEV void qk_role(const Params& p, int h, LAS unsigned char* L) {
  const int tid = threadIdx.x, lane = tid & 63, w = __builtin_amdgcn_readfirstlane(tid >> 6);
  const int nl = lane & 15, g = lane >> 4;
  const int wl = L_W + w * (NLDS * 2048) + lane * 16;
  unsigned* ctrl = (unsigned*)(p.ws + OFF_CTRL);
  if (tid < 4) *(LAS unsigned*)(L + L_FLAG + tid * 4) = 0u;
  constexpr int STG = L_MEM;
  WSet W;
  claim_agprs();
  load_proj<2, 1, 7>(p.wk, p.wq, h, w, lane, W, L, wl, STG, []() {});
  const rsrc_t rws = mkrsrc(p.ws, (unsigned)WS_END);
  const rsrc_t rc = mkrsrc(comm_base(p.ws), COMM_BYTES);
  const unsigned long long selm = load_selmask(p.ws, lane);
  const int idx = (nl < 8) ? 0x4444 : 0xEEEE;
  const int moff = g * 256 + ((nl >> 2) & 1) * 128 + (nl >> 3) * 64 + (nl & 3) * 16;
  const int tokX = 16 * ((nl >> 1) & 1) + 8 * (nl >> 3) + 2 * ((nl >> 2) & 1) + (nl & 1);
  u32x4 kX, kY; u32x2 kX2, kY2;
  unsigned kx_seen = 0;
  const int kxu = (tokX >> 4) * 1536, kxl = g * 16 + (tokX & 15);
  auto ldkx = [&](int s) {
    const int b = (int)OFF_KX + ((s * 32 + h) * 4 + w) * 3072;
    kX = __builtin_amdgcn_raw_buffer_load_b128(rws, kxu + kxl * 16, b, AUX_SC1);
    kX2 = __builtin_amdgcn_raw_buffer_load_b64(rws, kxu + 1024 + kxl * 8, b, AUX_SC1);
    kY = __builtin_amdgcn_raw_buffer_load_b128(rws, kxu + kxl * 16 + 64, b, AUX_SC1);
    kY2 = __builtin_amdgcn_raw_buffer_load_b64(rws, kxu + 1024 + kxl * 8 + 32, b, AUX_SC1);
    MEMFENCE();
  };
  auto load_kx = [&](int s) -> bool {
    if (!wait_prog(rws, pg_off(PG_KX + h * 4 + w), (unsigned)(s + 1), kx_seen, ctrl)) return false;
    ldkx(s);
    return true;
  };
  unsigned tflag = 0;
  int dpub = 92;
  const int ap_off = lane < 40 ? pg_off(PG_FILL2 + lane) : pg_off(PG_KX + h * 4 + w);
  if (!wait_words(rws, PG_FILL, 40, lane, ctrl)) return;
  kX = u32x4{0u, 0u, 0u, 0u}; kY = kX; kX2 = u32x2{0u, 0u}; kY2 = kX2;
  int qw = L_QK + w * 3584 + nl * 32 + g * 8, qr = L_QK + w * 3584 + g * 128 + nl * 8;
  PIN2(qw, qr);
  const int psw = L_PS + w * 512 + (g * 8 + (nl & 7)) * 16;
  const float cs = 0.07216878364870322f * 1.4426950408889634f;
  const unsigned tref = rt32();
  const unsigned char* cbp = comm_base(p.ws);
  unsigned fill_seen = 0, apv = 0, slack = 0;
  wg_barrier();
  for (int s_ = -1; s_ < NSEG; ++s_) {
    const int s = s_ < 0 ? 0 : s_;
    const int sb = step_base(selm, s), sbo = s_ < 0 ? sb + 48 * 16384 : sb;
    Ring8 ring;
    sfor<2 * NRING>([&](auto I_) { constexpr int I = decltype(I_)::v; ring_ld<I / 2, I & 1>(ring, L, wl); });
    u32x4 am[10];
    f32x4 acc[6], aX, aY, aM;
#pragma unroll
    for (int t = 0; t < 6; ++t) acc[t] = f32x4{0.f, 0.f, 0.f, 0.f};
    NOP_IN3(acc[0], acc[1], acc[2]);
    NOP_IN3(acc[3], acc[4], acc[5]);
    MemAddr ma;
    mem_addr(ma, w, moff);
    const bool have_kx = kx_seen >= (unsigned)(s_ + 2);
    const int kxb = (int)OFF_KX + (((have_kx ? s_ + 1 : s) * 32 + h) * 4 + w) * 3072;
    unsigned kmv = (unsigned)s;
    PIN1(kmv);
    if (s > 0) {
      unsigned tmax;
      if (!sched_get(L, s, tmax)) return;
      wait_rt(tmax + (unsigned)T_MEM + slack);
      slack = 0;
    }
    if (!gather_own(rc, sb, w, lane, moff, ma, L, ctrl, am[0], am[1], [&]() { slack = 8u; })) return;
    unsigned long long tb;
    sfor<12>([&](auto F_) {
      constexpr int F = decltype(F_)::v;
      smfr<F>(acc[F % 6], am[F / 6], W, ring, idx);
      if constexpr (F == 1) barrier_time(tb);
      if constexpr (F >= 1 && F <= 4) read_pair<F>(am, ma, L);
      if constexpr (F == 5) ring_ld<3, 0>(ring, L, wl);
      if constexpr (F == 6) ring_ld<3, 1>(ring, L, wl);
      if constexpr (F == 10) {
        time_wait(tb);
        tflag = (unsigned)tb;
        if (tid == 0) __builtin_amdgcn_raw_buffer_store_b32(nosent(tflag + (unsigned)dpub), rc, TSF_OFF + h * 128, sbo, AUX_SC1);
        MEMFENCE();
      }
      if constexpr (F == 11) ring_ld<4, 0>(ring, L, wl);
    });
    u32x4 qf, km; u32x2 qf2, km2;
    auto put = [&](int t) {
      PIN1(acc[t]);
      *(LAS u32x2*)(L + qw + t * 512) = u32x2{pack2(acc[t][0], acc[t][1]), pack2(acc[t][2], acc[t][3])};
      MEMFENCE();
    };
    auto get = [&](int t) { return __builtin_bit_cast(u32x2, __builtin_amdgcn_ds_read_tr16_b64_v4i16((LAS s16x4*)(L + qr + t * 512))); };
    unsigned pk0, pk1;
    auto cvt = [&](int t) { PIN1(acc[t]); pk0 = pack2(acc[t][0], acc[t][1]); pk1 = pack2(acc[t][2], acc[t][3]); PIN2(pk0, pk1); };
    auto wr = [&](int t) { *(LAS u32x2*)(L + qw + t * 512) = u32x2{pk0, pk1}; MEMFENCE(); };
    u32x2 qa, qb, ka, kb;
    sfor<48>([&](auto G_) {
      constexpr int F = 12 + decltype(G_)::v, t = (F - 12) / 8, T = 2 + (F - 12) % 8;
      smfr<F>(acc[t], am[T], W, ring, idx);
      ring_step<F, 12>(ring, L, wl);
      if constexpr (F == 12) ring_ld<4, 1>(ring, L, wl);
      if constexpr (F == 13) ring_ld<5, 0>(ring, L, wl);
      if constexpr (F == 14) ring_ld<5, 1>(ring, L, wl);
      if constexpr (F == 25) cvt(0);
      if constexpr (F == 26) wr(0);
      if constexpr (F == 29) { qa = get(0); MEMFENCE(); }
      if constexpr (F == 37) cvt(1);
      if constexpr (F == 38) { wr(1); qb = get(1); MEMFENCE(); }
      if constexpr (F == 41) cvt(2);
      if constexpr (F == 42) { wr(2); qf2 = get(2); MEMFENCE(); }
      if constexpr (F == 44) { qf = u32x4{qa.x, qa.y, qb.x, qb.y}; pin4(qf); }
      if constexpr (F == 45) MF0_VV(aX, kX, qf);
      if constexpr (F == 46) MF0_VV(aY, kY, qf);
      if constexpr (F == 49) { cvt(3); MF16_VV(aX, kX2, qf2); }
      if constexpr (F == 50) { wr(3); MF16_VV(aY, kY2, qf2); }
      if constexpr (F == 17) { __builtin_amdgcn_raw_buffer_store_b32(kmv, rws, 0, pg_off(PG_KM + h * 4 + w), AUX_SC1); MEMFENCE(); }
      if constexpr (F == 51) { kX = __builtin_amdgcn_raw_buffer_load_b128(rws, kxu + kxl * 16, kxb, AUX_SC1); MEMFENCE(); }
      if constexpr (F == 52) { kY = __builtin_amdgcn_raw_buffer_load_b128(rws, kxu + kxl * 16 + 64, kxb, AUX_SC1); MEMFENCE(); }
      if constexpr (F == 53) { ka = get(3); kX2 = __builtin_amdgcn_raw_buffer_load_b64(rws, kxu + 1024 + kxl * 8, kxb, AUX_SC1); MEMFENCE(); }
      if constexpr (F == 54) add_ror8_old(aX);
      if constexpr (F == 59) { kY2 = __builtin_amdgcn_raw_buffer_load_b64(rws, kxu + 1024 + kxl * 8 + 32, kxb, AUX_SC1); MEMFENCE(); }
      if constexpr (F == 55) cvt(4);
      if constexpr (F == 56) { wr(4); kb = get(4); MEMFENCE(); }
      if constexpr (F == 57) { lds_w128(L, psw, __builtin_bit_cast(u32x4, aX)); MEMFENCE(); }
      if constexpr (F == 58) add_ror8_old(aY);
    });
    lds_w128(L, psw + 2048, __builtin_bit_cast(u32x4, aY));
    asm volatile("s_nop 5" : "+v"(acc[5]) :: "memory");
    put(5);
    km2 = get(5);
    MEMFENCE();
    km = u32x4{ka.x, ka.y, kb.x, kb.y};
    asm volatile("s_nop 1" : "+v"(km));
    MF0_VV(aM, km, qf);
    asm volatile("s_nop 4" ::: "memory");
    MF16_VV(aM, km2, qf2);
    asm volatile("s_nop 5" : "+v"(aM));
    add_ror8(aM);
    lds_w128(L, psw + 4096, __builtin_bit_cast(u32x4, aM));
    wg_barrier();
    if (w == 0 && s < NSEG - 1) {
      const int i = lane >> 3, z = (lane >> 2) & 1, gq = lane & 3;
      f32x4 sx[4]; float sm[8];
#pragma unroll
      for (int k = 0; k < 8; ++k) sm[k] = *(LAS float*)(L + L_PS + 4096 + k * 256 + ((gq >> 1) * 8 + i) * 16 + (2 * z + (gq & 1)) * 4);
#pragma unroll
      for (int ww = 0; ww < 4; ++ww) sx[ww] = *(LAS f32x4*)(L + L_PS + z * 2048 + ww * 512 + (gq * 8 + i) * 16);
      float m1 = ((sm[0] + sm[1]) + (sm[2] + sm[3])) + ((sm[4] + sm[5]) + (sm[6] + sm[7]));
      f32x4 x4 = (sx[0] + sx[1]) + (sx[2] + sx[3]);
      x4 *= cs;
      m1 *= cs;
      PIN2(x4, m1);
      float mx = max3f(max3f(x4[0], x4[1], m1), x4[2], x4[3]);
      RED8("v_max_f32_dpp", mx);
#pragma unroll
      for (int r = 0; r < 4; ++r) x4[r] = __builtin_amdgcn_exp2f(x4[r] - mx);
      m1 = __builtin_amdgcn_exp2f(m1 - mx);
      float sum = ((x4[0] + x4[1]) + (x4[2] + x4[3])) + m1;
      RED8("v_add_f32_dpp", sum);
      const float inv = __builtin_amdgcn_rcpf(sum);
      const u32x2 px = {nosent(pack2(x4[0] * inv, x4[1] * inv)), nosent(pack2(x4[2] * inv, x4[3] * inv))};
      m1 *= inv;
      const float mn = __builtin_bit_cast(float, __builtin_amdgcn_update_dpp(0, __builtin_bit_cast(int, m1), 0xF5, 0xf, 0xf, true));
      const unsigned pm = nosent(pack2(m1, mn));
      const int pb = sbo + (CB_P + (h >> 3)) * 16384 + (h & 7) * 640;
      __builtin_amdgcn_raw_buffer_store_b64(px, rc, (i >> 2) * 256 + gq * 64 + (i & 3) * 16 + z * 8, pb, AUX_SC1);
      if (!(lane & 1)) __builtin_amdgcn_raw_buffer_store_b32(pm, rc, 512 + (i >> 2) * 64 + z * 32 + (i & 3) * 8 + (gq >> 1) * 4, pb, AUX_SC1);
      MEMFENCE();
      const int dt = (int)(rt32() - tflag);
      const int dn = s < 6 ? 16 : ((s & 7) ? 0 : 1), up = s < 6 ? 16 : 2;
      if (s_ >= 0) dpub = dt < dpub - dn ? dpub - dn : (dt > dpub + up ? dpub + up : dt);
    }
    {
      const int kb = (int)OFF_KM + ((s * 32 + h) * 4 + w) * 1536;
      st128(km, rws, lane * 16, kb, AUX_SC1);
      __builtin_amdgcn_raw_buffer_store_b64(km2, rws, lane * 8, kb + 1024, AUX_SC1);
    }
    if (s == NSEG - 1) { publish(rws, PG_KM + h * 4 + w, (unsigned)NSEG, lane); break; }
    if ((w == 1 || w == 2) && s_ >= 0 && s < NSEG - 1) {
      unsigned tmax;
      if (!flags_max16(rc, cbp, sb + TSF_OFF + (w - 1) * 2048, lane, tref, tmax)) { ctrl[0] = 4u; return; }
      sched_put(L, w - 1, s + 1, tmax);
    }
    seen_update(apv, LANES_FILL, fill_seen);
    seen_update(apv, LANES_PROD, kx_seen);
    if (!have_kx && s + 2 < NSEG) { if (!load_kx(s_ + 1)) return; }
    if (s_ < 0) wg_barrier();
    if (!wait_filled(rws, s + 1, fill_seen, lane, ctrl)) return;
    MEMFENCE();
    apv = __builtin_amdgcn_raw_buffer_load_b32(rws, ap_off, 0, AUX_SC1);
  }
}

DEV void v_role(const Params& p, int h, LAS unsigned char* L) {
  const int tid = threadIdx.x, lane = tid & 63, w = __builtin_amdgcn_readfirstlane(tid >> 6);
  const int nl = lane & 15, g = lane >> 4;
  const int wl = L_W + w * (NLDS * 2048) + lane * 16;
  unsigned* ctrl = (unsigned*)(p.ws + OFF_CTRL);
  if (tid < 4) *(LAS unsigned*)(L + L_FLAG + tid * 4) = 0u;
  constexpr int STG = L_MEM;
  WSet W;
  claim_agprs();
  load_vo<3, 30>(p.wv, p.wo, h, w, lane, W, L, wl, STG);
  const rsrc_t rws = mkrsrc(p.ws, (unsigned)WS_END);
  const rsrc_t rc = mkrsrc(comm_base(p.ws), COMM_BYTES);
  const unsigned long long selm = load_selmask(p.ws, lane);
  const int idx = (nl < 8) ? 0x4444 : 0xEEEE;
  const int moff = g * 256 + ((nl >> 2) & 1) * 128 + (nl >> 3) * 64 + (nl & 3) * 16;
  const int row0 = 4 * (g & 1) + 2 * (g >> 1);
  int voff[3];
#pragma unroll
  for (int t = 0; t < 3; ++t) {
    const int k = 48 * w + 16 * t + nl;
    voff[t] = L_QK + (k >> 6) * 1024 + (((k >> 4) & 3) * 16 + ((k >> 1) & 1) * 8 + row0) * 16 + ((k >> 2) & 3) * 4 + (k & 1) * 2;
  }
  const int ob_l = (g >> 1) * 16384 + nl * 16 + (g & 1) * 8;
  const unsigned tref = rt32();
  const unsigned char* cbp = comm_base(p.ws);
  unsigned fill_seen = 0, apv = 0, slack = 0;
  if (!wait_words(rws, PG_FILL, 40, lane, ctrl)) return;
  wg_barrier();
  for (int s = 0; s < NSEG; ++s) {
    const int sb = step_base(selm, s);
    Ring8 ring;
    sfor<2 * NRING>([&](auto I_) { constexpr int I = decltype(I_)::v; ring_ld<I / 2, I & 1>(ring, L, wl); });
    u32x4 am[10];
    f32x4 acc[3], o0[5], o1[5];
#pragma unroll
    for (int t = 0; t < 3; ++t) acc[t] = f32x4{0.f, 0.f, 0.f, 0.f};
    NOP_IN3(acc[0], acc[1], acc[2]);
#pragma unroll
    for (int i = 0; i < 5; ++i) { o0[i] = f32x4{0.f, 0.f, 0.f, 0.f}; o1[i] = o0[i]; PIN2(o0[i], o1[i]); }
    MemAddr ma;
    mem_addr(ma, w, moff);
    if (s > 0) {
      unsigned tmax;
      if (w == 1 || w == 2) {
        if (!flags_max16(rc, cbp, step_base(selm, s - 1) + TSF_OFF + (w - 1) * 2048, lane, tref, tmax)) { ctrl[0] = 5u; return; }
        sched_put(L, w - 1, s, tmax);
      }
      if (!sched_get(L, s, tmax)) return;
      wait_rt(tmax + (unsigned)T_MEM + slack);
      slack = 0;
    }
    if (!gather_own(rc, sb, w, lane, moff, ma, L, ctrl, am[0], am[1], [&]() { slack = 8u; })) return;
    sfor<6>([&](auto F_) {
      constexpr int F = decltype(F_)::v;
      smfr<F>(acc[F % 3], am[F / 3], W, ring, idx);
      if constexpr (F == 0) { wg_barrier(); read_pair<1>(am, ma, L); }
      if constexpr (F < 3) read_pair<F + 2>(am, ma, L);
      ring_step<F, 0>(ring, L, wl);
    });
    Epi e0, e1;
    sfor<24>([&](auto G_) {
      constexpr int F = 6 + decltype(G_)::v, t = (F - 6) / 8, T = 2 + (F - 6) % 8;
      smfr<F>(acc[t], am[T], W, ring, idx);
      ring_step<F, 0>(ring, L, wl);
      if constexpr (F == 18) epi1(e0, acc[0]);
      if constexpr (F == 21) epi2(e0);
      if constexpr (F == 22) epi3(e0, L, voff[0], 16);
      if constexpr (F == 26) epi1(e1, acc[1]);
      if constexpr (F == 29) { epi2(e1); epi3(e1, L, voff[1], 16); }
    });
    asm volatile("s_nop 3" : "+v"(acc[2]));
    epi1(e0, acc[2]);
    epi2(e0);
    epi3(e0, L, voff[2], 16);
    wg_barrier();
    u32x4 va[3];
#pragma unroll
    for (int T = 0; T < 3; ++T) va[T] = lds_r128(L, L_QK + T * 1024 + lane * 16);
    const int ob = sb + (CB_VOM + 10 * w) * 16384 + h * 256;
    float ea[5][4], eb[5][4];
    u32x2 eo[5];
    auto piece = [&](auto P_, auto K_) {
      constexpr int P = decltype(P_)::v, K = decltype(K_)::v;
      if constexpr (K < 2) {
        if constexpr (K == 0) PIN2(o0[P], o1[P]);
        const u32x4 x = __builtin_bit_cast(u32x4, o0[P]), y = __builtin_bit_cast(u32x4, o1[P]);
#pragma unroll
        for (int r = 2 * K; r < 2 * K + 2; ++r) {
          auto q = __builtin_amdgcn_permlane32_swap(x[r], y[r], false, false);
          ea[P][r] = __builtin_bit_cast(float, (unsigned)q[0]); eb[P][r] = __builtin_bit_cast(float, (unsigned)q[1]);
          PIN2(ea[P][r], eb[P][r]);
        }
      } else if constexpr (K == 2) {
#pragma unroll
        for (int r = 0; r < 4; ++r) ea[P][r] += eb[P][r];
        PIN2(ea[P][0], ea[P][1]); PIN2(ea[P][2], ea[P][3]);
      } else if constexpr (K == 3) {
        eo[P] = u32x2{pack2(ea[P][0], ea[P][1]), pack2(ea[P][2], ea[P][3])};
        PIN1(eo[P]);
      } else {
        __builtin_amdgcn_raw_buffer_store_b64(u32x2{nosent(eo[P].x), nosent(eo[P].y)}, rc, ob_l, ob + P * 32768, AUX_SC1);
        MEMFENCE();
      }
    };
    sfor<30>([&](auto N_) {
      constexpr int n = decltype(N_)::v, F = 30 + n, P = n / 6, Q = n % 6;
      smfr<F>((Q & 1) ? o1[P] : o0[P], va[Q / 2], W, ring, idx);
      ring_step<F, 0>(ring, L, wl);
      if constexpr (n >= 10 && (n - 10) % 6 < 5) piece(IC<(n - 10) / 6>{}, IC<(n - 10) % 6>{});
    });
    piece(IC<3>{}, IC<2>{}); piece(IC<3>{}, IC<3>{}); piece(IC<3>{}, IC<4>{});
    asm volatile("s_nop 1" : "+v"(o0[4]), "+v"(o1[4]));
    sfor<5>([&](auto K_) { piece(IC<4>{}, K_); });
    seen_update(apv, LANES_FILL, fill_seen);
    if (!wait_filled(rws, s + 1, fill_seen, lane, ctrl)) return;
    MEMFENCE();
    apv = __builtin_amdgcn_raw_buffer_load_b32(rws, pg_off(PG_FILL2 + (lane < 40 ? lane : 0)), 0, AUX_SC1);
  }
}

DEV float swap_add16(float a, float b) {
  auto r = __builtin_amdgcn_permlane16_swap(__builtin_bit_cast(unsigned, a), __builtin_bit_cast(unsigned, b), false, false);
  return __builtin_bit_cast(float, (unsigned)r[0]) + __builtin_bit_cast(float, (unsigned)r[1]);
}

DEV void col_role(const Params& p, int j, LAS unsigned char* L) {
  const int tid = threadIdx.x, lane = tid & 63, w = __builtin_amdgcn_readfirstlane(tid >> 6);
  const int nl = lane & 15, g = lane >> 4;
  unsigned* ctrl = (unsigned*)(p.ws + OFF_CTRL);
  const rsrc_t rws = mkrsrc(p.ws, (unsigned)WS_END);
  const rsrc_t rc = mkrsrc(comm_base(p.ws), COMM_BYTES);
  const unsigned long long selm = load_selmask(p.ws, lane);
  const int idx = (nl < 8) ? 0x4444 : 0xEEEE;
  const int half = nl >> 3, rg = (nl >> 2) & 1, ip = nl & 3;
  const unsigned tref = rt32();
  const unsigned char* cbp = comm_base(p.ws);
  unsigned fill_seen = 0, apv = 0;
  u32x8 vx[4];
  unsigned vox_seen = 0, tmax = 0;
  const int vox_w = pg_off(PG_VOX + (8 * w + (lane & 7)) * 4 + j / 10);
  auto load_vox = [&](int s) -> bool {
    if (!wait_prog(rws, vox_w, (unsigned)(s + 1), vox_seen, ctrl)) return false;
#pragma unroll
    for (int i = 0; i < 4; ++i) {
      const int b = (int)OFF_VOX + ((s * 40 + j) * 32 + 8 * w + 2 * i) * 1024;
      vx[i] = cat8(__builtin_amdgcn_raw_buffer_load_b128(rws, lane * 16, b, AUX_SC1), __builtin_amdgcn_raw_buffer_load_b128(rws, lane * 16 + 1024, b, AUX_SC1));
    }
    return true;
  };
  if (!wait_words(rws, PG_FILL, 40, lane, ctrl)) return;
  if (!load_vox(0)) return;
  const int px_l = (CB_P + w) * 16384 + (g >> 1) * 640 + rg * 256 + ((g & 1) * 2 + half) * 64 + ip * 16;
  const int pm_l = (CB_P + w) * 16384 + 2 * g * 640 + 512 + rg * 64 + half * 32 + ip * 8;
  const int vm_l = (CB_VOM + j) * 16384 + 8 * w * 256 + lane * 16;
  const int pub_l = (j >= 32 ? 8192 : 0) + j * 256 + lane * 4;
  const int row0 = 4 * (g & 1) + 2 * (g >> 1);
  for (int s = 0; s < NSEG - 1; ++s) {
    const int pb = step_base(selm, s);
    f32x4 a0;
    a0 = zero4();
    PIN1(a0);
    bool got = false;
    if (s > 0) {
      wait_rt(tmax + (unsigned)T_FLAG);
      got = flags_max8_scalar(cbp + (pb + TSF_OFF + 8 * w * 128), tref, tmax);
    }
    if (!got) { if (!flags_max<8>(rc, pb + TSF_OFF + 8 * w * 128, lane, tref, tmax)) { ctrl[0] = 6u; return; } }
    wait_rt(tmax + (unsigned)(got ? T_VM : T_P));
    for (unsigned spins = 0;; ++spins) {
      MEMFENCE();
      const u32x8 vm = cat8(__builtin_amdgcn_raw_buffer_load_b128(rc, vm_l, pb, AUX_SC1), __builtin_amdgcn_raw_buffer_load_b128(rc, vm_l + 1024, pb, AUX_SC1));
      MEMFENCE();
      if (spins == 0) wait_rt(tmax + (unsigned)T_P);
      MEMFENCE();
      u32x4 pa[4];
#pragma unroll
      for (int q = 0; q < 4; ++q) pa[q] = __builtin_amdgcn_raw_buffer_load_b128(rc, px_l + q * 1280, pb, AUX_SC1);
      const u32x2 pm0 = __builtin_amdgcn_raw_buffer_load_b64(rc, pm_l, pb, AUX_SC1);
      const u32x2 pm1 = __builtin_amdgcn_raw_buffer_load_b64(rc, pm_l + 640, pb, AUX_SC1);
      unsigned mv = umax4(umax4(0u, lo4(vm)), hi4(vm));
      PIN1(mv);
#pragma unroll
      for (int q = 0; q < 4; ++q) {
        SMF_V(a0, pa[q], vx[q], idx);
        pin4(pa[q]);
        mv = umax4(mv, pa[q]);
        PIN1(mv);
      }
      u32x4 pmA = {pm0.x, pm0.y, pm1.x, pm1.y};
      asm volatile("s_nop 1" : "+v"(pmA));
      SMF_V(a0, pmA, vm, idx);
      const bool ok = all_valid(umax4(mv, pmA));
      asm volatile("s_nop 4" : "+v"(a0));
      if (ok) break;
      if (spins > SPIN_CAP) { ctrl[0] = 2u; return; }
      a0 = zero4();
      PIN1(a0);
    }
    asm volatile("s_nop 1" : "+v"(a0));
    const float r0 = swap_add32(a0[0], a0[2]), r1 = swap_add32(a0[1], a0[3]);
    LAS float* red = (LAS float*)L;
    red[(w * 8 + row0) * 18 + nl] = r0;
    red[(w * 8 + row0 + 1) * 18 + nl] = r1;
    wg_barrier();
    if (w == 0) {
      const int i = 4 * (lane >> 5) + ((lane >> 2) & 3), hf = (lane >> 4) & 1, d = lane & 3;
      f32x2 pt[4];
#pragma unroll
      for (int ww = 0; ww < 4; ++ww) pt[ww] = *(LAS f32x2*)(red + (ww * 8 + i) * 18 + 4 * d + 2 * hf);
      const f32x2 sum = (pt[0] + pt[1]) + (pt[2] + pt[3]);
      __builtin_amdgcn_raw_buffer_store_b32(nosent(pack2(sum.x, sum.y)), rc, pub_l, step_base(selm, s + 1), AUX_SC1);
    }
    wg_barrier();
    seen_update(apv, LANES_PROD, vox_seen);
    if (s + 2 < NSEG) { if (!load_vox(s + 1)) return; }
    if (w == 0) {
      seen_update(apv, LANES_FILL, fill_seen);
      if (!wait_filled(rws, s + 2, fill_seen, lane, ctrl)) return;
    }
    MEMFENCE();
    apv = __builtin_amdgcn_raw_buffer_load_b32(rws, (w == 0 && lane < 40) ? pg_off(PG_FILL2 + lane) : vox_w, 0, AUX_SC1);
  }
}

template <int NS> struct HRing { u32x4 f[NS][2]; };
struct HOff { int v[2]; };
DEV HOff make_hoff(int lane) { HOff o; o.v[0] = lane * 16; o.v[1] = lane * 16 + 20480; PIN2(o.v[0], o.v[1]); return o; }
template <int NS> DEV void ldh1(HRing<NS>& hb, int slot, int mt, rsrc_t rws, const HOff& lo, int odd, int off) {
  hb.f[slot][mt] = __builtin_amdgcn_raw_buffer_load_b128(rws, lo.v[mt] + odd * 1024, off, 0);
}
constexpr int NL1 = 18;
DEV int hoff(int u, int h) { return rot_chunk(u >> 1, h) * 2048; }
template <int NS> DEV void hring_pin(HRing<NS>& hb) {
#pragma unroll
  for (int u = 0; u < NS - 1; ++u) PIN2(hb.f[u][0], hb.f[u][1]);
}
template <int NS> DEV void hring_init(HRing<NS>& hb, rsrc_t rws, const HOff& lo, int off, int h) {
#pragma unroll
  for (int u = 0; u < NS - 1; ++u) { ldh1(hb, u, 1, rws, lo, u & 1, off + hoff(u, h)); ldh1(hb, u, 0, rws, lo, u & 1, off + hoff(u, h)); }
  hring_pin(hb);
}
template <int N, int WORD2 = 0>
DEV void publish_late(rsrc_t rws, int word, unsigned val, int lane) {
  asm volatile("s_waitcnt vmcnt(%0)" :: "i"(N) : "memory");
  if (lane == 0) {
    __builtin_amdgcn_raw_buffer_store_b32(val, rws, 0, (int)OFF_PROG + word * 128, AUX_SC1);
    if (WORD2 != 0) __builtin_amdgcn_raw_buffer_store_b32(val, rws, 0, (int)OFF_PROG + (word + WORD2) * 128, AUX_SC1);
  }
}
constexpr int qx_pos(int F, int hi) {
  const int u = 2 * (F / 6) + hi, r = F % 6;
  return (r < 3 ? (u < 2 ? u : 2 * u - 1) : (u < 18 ? 2 * u + 2 : 20 + u)) * 3 + r % 3;
}
constexpr int QX_LEAD = 6, QX_D = 8;
constexpr int qx_at(int pos) {
  for (int F = 0; F < 60; ++F)
    if (f_lds(F, 2))
      for (int hi = 0; hi < 2; ++hi)
        if (qx_pos(F, hi) == pos) return 2 * F + hi;
  return -1;
}
template <int POS> DEV void qx_fetch(RingD (&lw)[18], LAS unsigned char* L, int wl) {
  constexpr int c = qx_at(POS + QX_LEAD);
  if constexpr (c >= 0) { constexpr int I = f_idx(c / 2, 2); lw[I].h[c & 1] = lds_r128(L, wl + I * 2048 + (c & 1) * 1024); }
}
template <int N> DEV void qx_piece(f32x4 (&a)[2][3], u32x4& o4, u32x2& o2, rsrc_t rws, int l16, int l8, int ob) {
  constexpr int mt = N / 6, k = N % 6;
  if constexpr (k == 0) { PIN1(a[mt][0]); o4.x = pack2(a[mt][0][0], a[mt][0][1]); o4.y = pack2(a[mt][0][2], a[mt][0][3]); pin4(o4); }
  if constexpr (k == 1) { PIN1(a[mt][1]); o4.z = pack2(a[mt][1][0], a[mt][1][1]); o4.w = pack2(a[mt][1][2], a[mt][1][3]); pin4(o4); }
  if constexpr (k == 2) st128(o4, rws, l16 + mt * 1536, ob, AUX_SC1);
  if constexpr (k == 3) { PIN1(a[mt][2]); o2 = u32x2{pack2(a[mt][2][0], a[mt][2][1]), pack2(a[mt][2][2], a[mt][2][3])}; PIN1(o2); }
  if constexpr (k == 4) { __builtin_amdgcn_raw_buffer_store_b64(o2, rws, l8 + mt * 1536, ob, AUX_SC1); MEMFENCE(); }
}
DEV void qkx_role(const Params& p, int h, LAS unsigned char* L) {
  const int tid = threadIdx.x, lane = tid & 63, w = __builtin_amdgcn_readfirstlane(tid >> 6);
  const int wl = L_W + w * (NL1 * 2048) + lane * 16;
  constexpr int STG = 4 * NL1 * 2048;
  WSetP<2> W;
  claim_agprs();
  load_proj<4, 5, 9>(p.wk, p.wq, h, w, lane, W, L, wl, STG, []() {});
  const rsrc_t rws = mkrsrc(p.ws, (unsigned)WS_END);
  if (h == 0 && tid == 0) __builtin_amdgcn_raw_buffer_store_b32(1u, rws, pg_off(PG_GO), 0, AUX_SC1);
  if (!wait_h(rws, lane, (unsigned*)(p.ws + OFF_CTRL))) return;
  const HOff lo = make_hoff(lane);
  int l8 = lane * 8 + 1024;
  PIN1(l8);
  HRing<QX_D + 1> hc;
  hring_init(hc, rws, lo, (int)OFF_HF, h);
  f32x4 aq[2][3];
#pragma unroll
  for (int i = 0; i < 6; ++i) aq[i / 3][i % 3] = f32x4{0.f, 0.f, 0.f, 0.f};
  u32x4 o4 = {0u, 0u, 0u, 0u};
  u32x2 o2;
  for (int s = 0; s < NSEG; ++s) {
    const int hf_off = (int)OFF_HF + s * 40960, nxt_off = (int)OFF_HF + ((s + 1) & 63) * 40960;
    const int kb = (int)OFF_KX + ((s * 32 + h) * 4 + w) * 3072;
    const int qb = (int)OFF_QX + (((s > 0 ? s - 1 : 0) * 32 + h) * 4 + w) * 3072;
    HRing<20 + QX_D> hb;
#pragma unroll
    for (int u = 0; u < QX_D; ++u) { hb.f[u][0] = hc.f[u][0]; hb.f[u][1] = hc.f[u][1]; }
    RingD lw[18];
    f32x4 ak[2][3];
    sfor<40>([&](auto G_) {
      constexpr int G = decltype(G_)::v;
      constexpr bool IS_K = G < 2 || (G < 38 && (G & 1));
      constexpr int u = IS_K ? (G < 2 ? G : (G + 1) / 2) : (G < 38 ? (G - 2) / 2 : G - 20), T = u / 2, un = u + QX_D;
      constexpr bool HI = (u & 1) != 0;
      const int noff = un < 20 ? hf_off + hoff(un, h) : nxt_off + hoff(un - 20, h);
      sfor<3>([&](auto t_) {
        constexpr int t = decltype(t_)::v, F = T * 6 + (IS_K ? 0 : 3) + t, LI = f_lds(F, 2) ? f_idx(F, 2) : 0;
        constexpr int e0 = G * 6 + 2 * t - 230;
        mfw<F, true, HI, u == 0>(IS_K ? ak[0][t] : aq[0][t], hb.f[u][0], W, lw[LI]);
        if constexpr (!IS_K && t < 2) ldh1(hb, un, 1 - t, rws, lo, un & 1, noff);
        if constexpr (G < 2) qx_piece<G * 6 + 2 * t>(aq, o4, o2, rws, lo.v[0], l8, qb);
        if constexpr (e0 >= 0) qx_piece<(e0 >= 0 ? e0 : 5)>(ak, o4, o2, rws, lo.v[0], l8, kb);
        mfw<F, true, HI, u == 0>(IS_K ? ak[1][t] : aq[1][t], hb.f[u][1], W, lw[LI]);
        if constexpr (G < 2) qx_piece<G * 6 + 2 * t + 1>(aq, o4, o2, rws, lo.v[0], l8, qb);
        if constexpr (e0 >= 0) qx_piece<(e0 >= 0 ? e0 + 1 : 5)>(ak, o4, o2, rws, lo.v[0], l8, kb);
        qx_fetch<G * 3 + t>(lw, L, wl);
      });
      if constexpr (G == 13) { if (s > 0) publish_late<12, PG_QX - PG_KX>(rws, PG_KX + h * 4 + w, (unsigned)s, lane); }
    });
    qx_piece<10>(ak, o4, o2, rws, lo.v[0], l8, kb);
    asm volatile("s_nop 4" ::: "memory");
#pragma unroll
    for (int u = 0; u < QX_D; ++u) { hc.f[u][0] = hb.f[20 + u][0]; hc.f[u][1] = hb.f[20 + u][1]; }
    if (s == 0) publish(rws, PG_KX + h * 4 + w, 1u, lane);
  }
  asm volatile("s_nop 7" ::: "memory");
  {
    const int qb = (int)OFF_QX + (((NSEG - 1) * 32 + h) * 4 + w) * 3072;
    sfor<12>([&](auto N_) { qx_piece<decltype(N_)::v>(aq, o4, o2, rws, lo.v[0], l8, qb); });
    publish_late<0, PG_QX - PG_KX>(rws, PG_KX + h * 4 + w, (unsigned)NSEG, lane);
  }
}

DEV void px_role(const Params& p, int id, LAS unsigned char* L) {
  const int tid = threadIdx.x, lane = tid & 63, w = __builtin_amdgcn_readfirstlane(tid >> 6);
  const int nl = lane & 15, g = lane >> 4;
  const int h = id * 2 + (w >> 1), mt = w & 1;
  unsigned* ctrl = (unsigned*)(p.ws + OFF_CTRL);
  const rsrc_t rws = mkrsrc(p.ws, (unsigned)WS_END);
  const float cs = 0.07216878364870322f * 1.4426950408889634f;
  unsigned kx_seen = 0, km_seen = 0;
  Pend pd = {pg_off(PG_PX + h * 2 + mt), 0u, false};
  auto ld6 = [&](u32x4 (&f)[6], int base, int wstride) {
#pragma unroll
    for (int u = 0; u < 4; ++u) f[u] = __builtin_amdgcn_raw_buffer_load_b128(rws, lane * 16, base + u * wstride, AUX_SC1);
#pragma unroll
    for (int u = 0; u < 2; ++u) {
      const u32x2 a = __builtin_amdgcn_raw_buffer_load_b64(rws, lane * 8, base + (2 * u) * wstride + 1024, AUX_SC1);
      const u32x2 b = __builtin_amdgcn_raw_buffer_load_b64(rws, lane * 8, base + (2 * u + 1) * wstride + 1024, AUX_SC1);
      f[4 + u] = u32x4{a.x, a.y, b.x, b.y};
    }
  };
  u32x4 qn[6], kX[6], kY[6];
  auto ld_next = [&](int s) -> bool {
    if (!wait_prog_pub(rws, pg_off(PG_QX + h * 4 + (lane & 3)), (unsigned)(s + 1), kx_seen, ctrl, pd, lane)) return false;
    const int xb = (s * 32 + h) * 12288;
    ld6(qn, (int)OFF_QX + xb + mt * 1536, 3072);
    ld6(kX, (int)OFF_KX + xb, 3072);
    ld6(kY, (int)OFF_KX + xb + 1536, 3072);
    return true;
  };
  if (!ld_next(0)) return;
  for (int s = 0; s < NSEG; ++s) {
    {
      u32x4 qf[6], km[6];
      f32x4 aX, aY, aM;
#pragma unroll
      for (int u = 0; u < 6; ++u) { qf[u] = qn[u]; PIN1(qf[u]); PIN2(kX[u], kY[u]); }
      asm volatile("s_nop 1" ::: "memory");
      MF0_VV(aX, kX[0], qf[0]); MF0_VV(aY, kY[0], qf[0]);
#pragma unroll
      for (int u = 1; u < 6; ++u) { MF_VV(aX, kX[u], qf[u]); MF_VV(aY, kY[u], qf[u]); }
      pend_after_loads<0>(rws, pd, lane);
      if (s + 1 < NSEG) { if (!ld_next(s + 1)) return; }
      if (!wait_prog<1>(rws, pg_off(PG_KM + h * 4 + (lane & 3)), (unsigned)(s + 1), km_seen, ctrl)) return;
      ld6(km, (int)OFF_KM + (s * 32 + h) * 6144, 1536);
#pragma unroll
      for (int u = 0; u < 6; ++u) PIN1(km[u]);
      asm volatile("s_nop 1" ::: "memory");
      MF0_VV(aM, km[0], qf[0]);
#pragma unroll
      for (int u = 1; u < 6; ++u) MF_VV(aM, km[u], qf[u]);
      NOP_OUT3(aX, aY, aM);
      const int tok = mt * 16 + nl;
#pragma unroll
      for (int r = 0; r < 4; ++r) {
        aM[r] = swap_add32(aM[r], aM[r]);
        if (4 * g + r > tok) aX[r] = -INFINITY;
        if (16 + 4 * g + r > tok) aY[r] = -INFINITY;
        if (g >= 2) aM[r] = -INFINITY;
      }
      float mx = fmaxf(fmaxf(fmaxf(aX[0], aX[1]), fmaxf(aX[2], aX[3])), fmaxf(fmaxf(aY[0], aY[1]), fmaxf(aY[2], aY[3])));
      mx = fmaxf(mx, fmaxf(fmaxf(aM[0], aM[1]), fmaxf(aM[2], aM[3])));
      mx = max_g(mx);
      float sum = 0.f;
#pragma unroll
      for (int r = 0; r < 4; ++r) {
        aX[r] = __builtin_amdgcn_exp2f((aX[r] - mx) * cs);
        aY[r] = __builtin_amdgcn_exp2f((aY[r] - mx) * cs);
        aM[r] = __builtin_amdgcn_exp2f((aM[r] - mx) * cs);
        sum += aX[r] + aY[r] + aM[r];
      }
      sum = sum_g(sum);
      const float inv = __builtin_amdgcn_rcpf(sum);
      const u32x4 px = {pack2(aX[0] * inv, aX[1] * inv), pack2(aX[2] * inv, aX[3] * inv), pack2(aY[0] * inv, aY[1] * inv), pack2(aY[2] * inv, aY[3] * inv)};
      const u32x2 pm = {pack2(aM[0] * inv, aM[1] * inv), pack2(aM[2] * inv, aM[3] * inv)};
      const int pb = (int)OFF_PX + (s * 2 + mt) * 40960;
      st128(px, rws, lane * 16, pb + h * 1024, AUX_SC1);
      if (g < 2) __builtin_amdgcn_raw_buffer_store_b64(pm, rws, pb + (32 + (h >> 2)) * 1024 + ((h & 3) * 16 + nl) * 16 + g * 8, 0, AUX_SC1);
      MEMFENCE();
      pd.val = (unsigned)(s + 1); pd.on = true;
      if (s == NSEG - 1) pend_after_loads<0>(rws, pd, lane);
    }
  }
}

constexpr int L_VV = 4 * 18 * 2048;
static_assert(L_VV + 14336 <= L_FLAG, "lds");
constexpr int vx_pos(int F, int hi) {
  const int T = F / 6, r = F % 6;
  if (r < 3) { const int u = 2 * T + hi; return (u < 2 ? u : 2 * u - 1) * 3 + r; }
  const int k = 2 * (r - 3) + hi, x = 2 * T + (k >= 3 ? 1 : 0);
  return (x < 18 ? 2 + 2 * x : 20 + x) * 3 + k % 3;
}
constexpr int VX_LEAD = 6;
constexpr int vx_at(int pos) {
  for (int F = 0; F < 60; ++F)
    if (f_lds(F, 2))
      for (int hi = 0; hi < 2; ++hi)
        if (vx_pos(F, hi) == pos) return 2 * F + hi;
  return -1;
}
template <int POS> DEV void vx_fetch(RingD (&lw)[18], LAS unsigned char* L, int wl) {
  constexpr int c = vx_at(POS + VX_LEAD);
  if constexpr (c >= 0) { constexpr int I = f_idx(c / 2, 2); lw[I].h[c & 1] = lds_r128(L, wl + I * 2048 + (c & 1) * 1024); }
}
constexpr int VOX_D = 7;
template <bool DO_O>
DEV void vox_iter(int s, int h, int w, int lane, const HOff& lo, HRing<VOX_D + 1>& hc, u32x4 (&va)[2][6], int vrd, int vwr, rsrc_t rws,
                  const WSetP<2>& W, LAS unsigned char* L, int wl) {
  const int hf_off = (int)OFF_HF + ((s + 1) & 63) * 40960, nxt_off = (int)OFF_HF + ((s + 2) & 63) * 40960;
  HRing<20 + VOX_D> hb;
#pragma unroll
  for (int u = 0; u < VOX_D; ++u) { hb.f[u][0] = hc.f[u][0]; hb.f[u][1] = hc.f[u][1]; }
  auto ldv = [&](int k) { va[k & 1][k >> 1] = lds_r128(L, vrd + (k & 1) * 16 * QROW + (k >> 1) * 64); };
  f32x4 acc[2][3], o[2][2];
  RingD lw[18];
  u32x4 ov;
  auto cvt_half = [&](f32x4& oo, int z) {
    PIN1(oo);
    ov[2 * z] = pack2(oo[0], oo[1]); ov[2 * z + 1] = pack2(oo[2], oo[3]);
    pin4(ov);
  };
  auto store_tile = [&](int t) { st128(ov, rws, lo.v[0], (int)OFF_VOX + ((s * 40 + 10 * w + t) * 32 + h) * 1024, AUX_SC1); };
  auto put_v = [&](int i) {
    f32x4& a = acc[i & 1][i >> 1];
    PIN1(a);
    *(LAS u32x2*)(L + vwr + (i & 1) * 16 * QROW + 32 * (i >> 1)) = u32x2{pack2(a[0], a[1]), pack2(a[2], a[3])};
    MEMFENCE();
  };
  sfor<40>([&](auto G_) {
    constexpr int G = decltype(G_)::v;
    constexpr bool IS_P = G < 2 || (G < 38 && (G & 1));
    if constexpr (IS_P) {
      constexpr int u = G < 2 ? G : (G + 1) / 2, T = u / 2;
      constexpr bool HI = (u & 1) != 0, CV = DO_O && HI && u >= 3;
      const int noff = u + VOX_D < 20 ? hf_off + hoff(u + VOX_D, h) : nxt_off + hoff(u + VOX_D - 20, h);
      sfor<3>([&](auto t_) {
        constexpr int t = decltype(t_)::v, F = T * 6 + t, LI = f_lds(F, 2) ? f_idx(F, 2) : 0;
        mfw<F, true, HI, u == 0>(acc[0][t], hb.f[u][0], W, lw[LI]);
        if constexpr (t < 2) ldh1(hb, u + VOX_D, 1 - t, rws, lo, (u + VOX_D) & 1, noff);
        if constexpr (CV && t == 2) cvt_half(o[((u - 3) / 2) & 1][0], 0);
        mfw<F, true, HI, u == 0>(acc[1][t], hb.f[u][1], W, lw[LI]);
        if constexpr (CV && t == 2) cvt_half(o[((u - 3) / 2) & 1][1], 1);
        if constexpr (DO_O && u < 2) ldv(6 + 3 * u + t);
        vx_fetch<G * 3 + t>(lw, L, wl);
      });
    } else {
      constexpr int x = G < 38 ? (G - 2) / 2 : G - 20, T = x / 2;
      constexpr bool HI = (x & 1) != 0;
      sfor<3>([&](auto k_) {
        constexpr int kk = decltype(k_)::v, k = kk + (HI ? 3 : 0), F = T * 6 + 3 + k / 2, LI = f_lds(F, 2) ? f_idx(F, 2) : 0;
        if constexpr (DO_O) {
          mfw<F, false, (k & 1) != 0, k == 0>(o[T & 1][0], va[0][k], W, lw[LI]);
          if constexpr (!HI && T > 0 && kk == 0) store_tile(T - 1);
          if constexpr (x == 18 && kk > 0) put_v(2 * kk - 2);
          if constexpr (x == 19 && kk == 0) put_v(4);
          mfw<F, false, (k & 1) != 0, k == 0>(o[T & 1][1], va[1][k], W, lw[LI]);
          if constexpr (x == 18 && kk > 0) put_v(2 * kk - 1);
          if constexpr (x == 19 && kk == 0) put_v(5);
        }
        vx_fetch<G * 3 + kk>(lw, L, wl);
      });
    }
    if constexpr (DO_O && G == 12) { if (s > 1) publish_late<14>(rws, PG_VOX + h * 4 + w, (unsigned)s, lane); }
    if constexpr (DO_O && G == 20) wg_barrier();
  });
  if constexpr (!DO_O) {
    asm volatile("s_nop 7" ::: "memory");
#pragma unroll
    for (int i = 0; i < 6; ++i) put_v(i);
  }
  wg_barrier();
#pragma unroll
  for (int k = 0; k < 6; ++k) ldv(k);
  MEMFENCE();
  if constexpr (DO_O) { cvt_half(o[1][0], 0); cvt_half(o[1][1], 1); store_tile(9); }
#pragma unroll
  for (int u = 0; u < VOX_D; ++u) { hc.f[u][0] = hb.f[20 + u][0]; hc.f[u][1] = hb.f[20 + u][1]; }
}
DEV void vox_role(const Params& p, int h, LAS unsigned char* L) {
  const int tid = threadIdx.x, lane = tid & 63, w = __builtin_amdgcn_readfirstlane(tid >> 6);
  const int wl = L_W + w * (18 * 2048) + lane * 16;
  constexpr int STG = L_VV;
  WSetP<2> W;
  claim_agprs();
  load_vo<4, -1>(p.wv, p.wo, h, w, lane, W, L, wl, STG);
  wg_barrier();
  const rsrc_t rws = mkrsrc(p.ws, (unsigned)WS_END);
  if (!wait_h(rws, lane, (unsigned*)(p.ws + OFF_CTRL))) return;
  HRing<VOX_D + 1> hb;
  const HOff lo = make_hoff(lane);
  hring_init(hb, rws, lo, (int)OFF_HF, h);
  int vrd = L_VV + (lane & 15) * QROW + (lane >> 4) * 16, vwr = L_VV + (lane & 15) * QROW + 96 * w + (lane >> 4) * 8;
  PIN2(vrd, vwr);
  u32x4 va[2][6];
  vox_iter<false>(-1, h, w, lane, lo, hb, va, vrd, vwr, rws, W, L, wl);
  hring_pin(hb);
  for (int s = 0; s < NSEG; ++s) {
    vox_iter<true>(s, h, w, lane, lo, hb, va, vrd, vwr, rws, W, L, wl);
    if (s == 0 || s == NSEG - 1) publish(rws, PG_VOX + h * 4 + w, (unsigned)(s + 1), lane);
  }
}

DEV int col_of(int k) { const int j = k >> 4; return 160 * (j / 10) + 10 * (k & 15) + j % 10; }

DEV void xcol_role(const Params& p, int cg, int par, LAS unsigned char* L) {
  const int tid = threadIdx.x, lane = tid & 63, w = __builtin_amdgcn_readfirstlane(tid >> 6);
  const int nl = lane & 15, g = lane >> 4;
  unsigned* ctrl = (unsigned*)(p.ws + OFF_CTRL);
  const rsrc_t rws = mkrsrc(p.ws, (unsigned)WS_END);
  const rsrc_t rc = mkrsrc(comm_base(p.ws), COMM_BYTES);
  const unsigned long long selm = load_selmask(p.ws, lane);
  const int j0 = 2 * cg;
  const int c0 = col_of(32 * cg + 8 * g);
  if (!wait_words(rws, PG_MOD, 40, lane, ctrl)) return;
  if (!wait_words(rws, PG_FILL, 40, lane, ctrl)) return;
  float ga[8];
#pragma unroll
  for (int i = 0; i < 8; ++i) ga[i] = ldmod(rws, 2 * D_ + c0 + 10 * i);
  unsigned vs = 0, ps = 0;
  const int vox_w = pg_off(PG_VOX + (8 * w + (lane & 7)) * 4 + j0 / 10);
  const int px_w = pg_off(PG_PX + 16 * w + (lane & 15));
  for (int s = par; s < NSEG; s += 2) {
    const unsigned want = (unsigned)(s + 1);
    {
      const int fs = NEARLY + (s & ~1), xid = cg * 2 + par;
      if (fs < NSEG) {
#pragma unroll
        for (int z = 0; z < 2; ++z) {
          fill_blk(rc, step_base(selm, fs + z) + xid * 16384, tid);
          if (xid + 40 < CB_N) fill_blk(rc, blk_off(step_base(selm, fs + z), xid + 40), tid);
        }
      }
    }
    if (!wait_prog(rws, vox_w, want, vs, ctrl)) return;
    u32x4 b[2][10], a[2][10];
#pragma unroll
    for (int jt = 0; jt < 2; ++jt)
#pragma unroll
      for (int i = 0; i < 8; ++i)
        b[jt][i] = __builtin_amdgcn_raw_buffer_load_b128(rws, lane * 16, (int)OFF_VOX + ((s * 40 + j0 + jt) * 32 + 8 * w + ((i + (cg >> 1)) & 7)) * 1024, AUX_SC1);
    u16 xv[8];
    if (w < 2) {
#pragma unroll
      for (int i = 0; i < 8; ++i) xv[i] = p.x[(size_t)(s * 32 + w * 16 + nl) * D_ + c0 + 10 * i];
    }
    if (!wait_prog<1>(rws, px_w, want, ps, ctrl)) return;
#pragma unroll
    for (int i = 0; i < 10; ++i)
#pragma unroll
      for (int mt = 0; mt < 2; ++mt) {
        const int step = i < 8 ? 8 * w + ((i + (cg >> 1)) & 7) : 32 + 2 * w + (i - 8);
        a[mt][i] = __builtin_amdgcn_raw_buffer_load_b128(rws, lane * 16, (int)OFF_PX + ((s * 2 + mt) * 40 + step) * 1024, AUX_SC1);
      }
    {
      const int vmo = step_base(selm, s) + (CB_VOM + j0) * 16384 + 2 * w * 1024;
      unsigned spins = 0;
      bool ok;
      do {
        MEMFENCE();
#pragma unroll
        for (int q = 0; q < 4; ++q) b[q >> 1][8 + (q & 1)] = __builtin_amdgcn_raw_buffer_load_b128(rc, lane * 16, vmo + (q >> 1) * 16384 + (q & 1) * 1024, AUX_SC1);
        ok = all_valid(umax4(umax4(umax4(umax4(0u, b[0][8]), b[0][9]), b[1][8]), b[1][9]));
      } while (!ok && ++spins < SPIN_CAP);
      if (!ok) { ctrl[0] = 7u; return; }
    }
    f32x4 acc[2][2];
#pragma unroll
    for (int i = 0; i < 10; ++i)
#pragma unroll
      for (int q = 0; q < 4; ++q) {
        if (i == 0) MF0_VV(acc[q >> 1][q & 1], b[q & 1][0], a[q >> 1][0]);
        else MF_VV(acc[q >> 1][q & 1], b[q & 1][i], a[q >> 1][i]);
      }
    asm volatile("s_nop 7" : "+v"(acc[0][0]), "+v"(acc[0][1]), "+v"(acc[1][0]), "+v"(acc[1][1]));
    LAS float* red = (LAS float*)(L + ((s >> 1) & 1) * 20480);
#pragma unroll
    for (int q = 0; q < 4; ++q) *(LAS f32x4*)(red + ((w * 2 + (q >> 1)) * 16 + nl) * 36 + (q & 1) * 16 + 4 * g) = acc[q >> 1][q & 1];
    asm volatile("s_waitcnt vmcnt(0)" ::: "memory");
    wg_barrier();
    if (tid == 128) __builtin_amdgcn_raw_buffer_store_b32((unsigned)((s >> 1) + 1), rws, pg_off(PG_FILL2 + cg * 2 + par), 0, AUX_SC1);
    if (w < 2) {
      f32x4 s0 = {0.f, 0.f, 0.f, 0.f}, s1 = s0;
#pragma unroll
      for (int ww = 0; ww < 4; ++ww) {
        s0 += *(LAS f32x4*)(red + ((ww * 2 + w) * 16 + nl) * 36 + 8 * g);
        s1 += *(LAS f32x4*)(red + ((ww * 2 + w) * 16 + nl) * 36 + 8 * g + 4);
      }
      const u32x4 o = {nosent(pack2(bf2f(xv[0]) + ga[0] * s0[0], bf2f(xv[1]) + ga[1] * s0[1])), nosent(pack2(bf2f(xv[2]) + ga[2] * s0[2], bf2f(xv[3]) + ga[3] * s0[3])),
                       nosent(pack2(bf2f(xv[4]) + ga[4] * s1[0], bf2f(xv[5]) + ga[5] * s1[1])), nosent(pack2(bf2f(xv[6]) + ga[6] * s1[2], bf2f(xv[7]) + ga[7] * s1[3]))};
      st128(o, rc, lane * 16, frag_off(step_base(selm, s), FR_X1 + w * 20 + cg), AUX_SC1);
    }
    wg_barrier();
  }
}

DEV float dot2(unsigned a, unsigned b, float c) {
  return __builtin_amdgcn_fdot2_f32_bf16(__builtin_bit_cast(bf16x2, a), __builtin_bit_cast(bf16x2, b), c, false);
}
DEV float gelu_tanh(float x) {
  const float u = 0.7978845608028654f * (x + 0.044715f * x * x * x);
  return x * __builtin_amdgcn_rcpf(1.f + __builtin_amdgcn_exp2f(-2.885390081777927f * u));
}
DEV void ffn1_role(const Params& p, int fg, int mt, LAS unsigned char* L) {
  const int tid = threadIdx.x, lane = tid & 63, w = __builtin_amdgcn_readfirstlane(tid >> 6);
  const int nl = lane & 15, g = lane >> 4;
  unsigned* ctrl = (unsigned*)(p.ws + OFF_CTRL);
  const rsrc_t rws = mkrsrc(p.ws, (unsigned)WS_END);
  claim_agprs();
  f32x2 ma[5][4], mb[5][4];
  if (!wait_words(rws, PG_MOD, 40, lane, ctrl)) return;
#pragma unroll
  for (int i = 0; i < 5; ++i)
#pragma unroll
    for (int e = 0; e < 4; ++e) {
      const int c = col_of(32 * (5 * w + i) + 8 * g) + 20 * e;
      mb[i][e] = f32x2{ldmod(rws, 3 * D_ + c), ldmod(rws, 3 * D_ + c + 10)};
      ma[i][e] = f32x2{ldmod(rws, 4 * D_ + c), ldmod(rws, 4 * D_ + c + 10)};
      if (e == 3 && (i == 2 || i == 4)) MEMFENCE();
    }
#pragma unroll
  for (int i = 0; i < 5; ++i)
#pragma unroll
    for (int e = 0; e < 4; ++e) { ma[i][e] += f32x2{1.f, 1.f}; PIN2(ma[i][e], mb[i][e]); }
  u32x4 wv[4][4];
  {
    const rsrc_t rg = mkrsrc(p.wg, D_ * DF * 2), ru = mkrsrc(p.wu, D_ * DF * 2);
    int voff[2], sw[2];
#pragma unroll
    for (int i = 0; i < 2; ++i) {
      const int r = (lane >> 2) + 16 * i;
      voff[i] = ((lane_row<true>(r >> 3) + 10 * (r & 7)) * DF + 128 * fg + 32 * w) * 2 + (lane & 3) * 16;
      sw[i] = w * 5120 + r * 80 + (lane & 3) * 16;
    }
    const int sr = w * 5120 + (g * 8 + (nl >> 2)) * 80 + (nl & 3) * 16;
    const unsigned sra = (unsigned)(uintptr_t)(L + sr);
    constexpr int DP = 5;
    u32x4 raw[DP][4];
    auto issue = [&](int u, u32x4 (&r)[4]) {
#pragma unroll
      for (int i = 0; i < 4; ++i) r[i] = __builtin_amdgcn_raw_buffer_load_b128(i < 2 ? rg : ru, voff[i & 1], base_row<true>(u) * DF * 2, 0);
    };
#pragma unroll
    for (int u = 0; u < DP - 1; ++u) issue(u, raw[u]);
    sfor<20>([&](auto U_) {
      constexpr int u = decltype(U_)::v;
      if constexpr (u + DP - 1 < 20) issue(u + DP - 1, raw[(u + DP - 1) % DP]);
      MEMFENCE();
#pragma unroll
      for (int i = 0; i < 4; ++i) lds_w128(L, sw[i & 1] + (i >> 1) * 2560, raw[u % DP][i]);
      MEMFENCE();
      sfor<4>([&](auto Q_) {
        constexpr int q = decltype(Q_)::v, off = (q >> 1) * 2560 + (q & 1) * 8;
        if constexpr (u < 16) tr_to_agpr<(u * 4 + q) * 4, off, 320>(sra);
        else { wv[u - 16][q] = tr_frag<320>(L, sr + off); pin4(wv[u - 16][q]); }
      });
      MEMFENCE();
    });
    wg_barrier();
  }
  const rsrc_t rc = mkrsrc(comm_base(p.ws), COMM_BYTES);
  const unsigned long long selm = load_selmask(p.ws, lane);
  LAS float* st = (LAS float*)(L + 49152);
  u32x4 xr[5];
  auto ldx = [&](int s) {
#pragma unroll
    for (int i = 0; i < 5; ++i) xr[i] = __builtin_amdgcn_raw_buffer_load_b128(rc, lane * 16, frag_off(step_base(selm, s), FR_X1 + mt * 20 + 5 * w + i), AUX_SC1);
  };
  if (!wait_words(rws, PG_FILL, 40, lane, ctrl)) return;
  ldx(0);
  for (int s = 0; s < NSEG; ++s) {
    for (unsigned spins = 0; !all_valid(umax4(umax4(umax4(umax4(umax4(0u, xr[0]), xr[1]), xr[2]), xr[3]), xr[4])); ++spins) {
      if (spins > SPIN_CAP) { ctrl[0] = 10u; return; }
      __builtin_amdgcn_s_sleep(3);
      MEMFENCE();
      ldx(s);
    }
    float s1 = 0.f, s2 = 0.f, t1 = 0.f, t2 = 0.f;
#pragma unroll
    for (int i = 0; i < 5; ++i)
#pragma unroll
      for (int e = 0; e < 4; e += 2) {
        s1 = dot2(xr[i][e], 0x3F803F80u, s1); s2 = dot2(xr[i][e], xr[i][e], s2);
        t1 = dot2(xr[i][e + 1], 0x3F803F80u, t1); t2 = dot2(xr[i][e + 1], xr[i][e + 1], t2);
      }
    float sg = swap_add32(s1 + t1, s2 + t2);
    sg = swap_add16(sg, sg);
    LAS float* sb = st + (s & 1) * 128;
    if (!(g & 1)) sb[(nl * 2 + (g >> 1)) * 4 + w] = sg;
    wg_barrier();
    const f32x4 q1 = *(LAS f32x4*)(sb + nl * 8), q2 = *(LAS f32x4*)(sb + nl * 8 + 4);
    const float mean = ((q1.x + q1.y) + (q1.z + q1.w)) * (1.f / D_);
    const float var = fmaxf(((q2.x + q2.y) + (q2.z + q2.w)) * (1.f / D_) - mean * mean, 0.f);
    const float rstd = __builtin_amdgcn_rsqf(var + 1e-5f);
    const f32x2 r2 = {rstd, rstd}, n2 = {-mean * rstd, -mean * rstd};
    const int hb = (s & 1) * 20480;
#pragma unroll
    for (int i = 0; i < 5; ++i) {
      u32x4 o;
#pragma unroll
      for (int e = 0; e < 4; ++e) {
        const f32x2 xv = {bflo(xr[i][e]), bfhi(xr[i][e])};
        const f32x2 y = __builtin_elementwise_fma(__builtin_elementwise_fma(xv, r2, n2), ma[i][e], mb[i][e]);
        o[e] = pack2(y.x, y.y);
      }
      lds_w128(L, hb + (5 * w + i) * 1024 + lane * 16, o);
    }
    wg_barrier();
    f32x4 acc[4];
    u32x4 hf[6];
#pragma unroll
    for (int u = 0; u < 5; ++u) hf[u] = lds_r128(L, hb + u * 1024 + lane * 16);
    sfor<20>([&](auto U_) {
      constexpr int u = decltype(U_)::v;
      if constexpr (u + 5 < 20) hf[(u + 5) % 6] = lds_r128(L, hb + (u + 5) * 1024 + lane * 16);
      sfor<4>([&](auto Q_) {
        constexpr int q = decltype(Q_)::v;
        if constexpr (u < 16) mfa<(u * 4 + q) * 4, u == 0>(acc[q], hf[u % 6]);
        else mfv(acc[q], wv[u - 16][q], hf[u % 6]);
      });
      if constexpr (u == 13) { MEMFENCE(); ldx(s + 1 < NSEG ? s + 1 : s); MEMFENCE(); }
    });
    asm volatile("s_nop 7" : "+v"(acc[0]), "+v"(acc[1]), "+v"(acc[2]), "+v"(acc[3]));
    float o[8];
#pragma unroll
    for (int r = 0; r < 4; ++r) { o[r] = gelu_tanh(acc[0][r]) * acc[2][r]; o[4 + r] = gelu_tanh(acc[1][r]) * acc[3][r]; }
#pragma unroll
    for (int i = 0; i < 5; ++i) pin4(xr[i]);
    st128(u32x4{nosent(pack2(o[0], o[1])), nosent(pack2(o[2], o[3])), nosent(pack2(o[4], o[5])), nosent(pack2(o[6], o[7]))}, rc,
          lane * 16, frag_off(step_base(selm, s), FR_ACT + mt * 48 + 4 * fg + w), AUX_SC1);
  }
}

constexpr int d_loc(int u, int t) { return (t < 5 || (t == 5 && u < 4)) ? LOC_A : ((t < 7 || (t == 7 && u < 8)) ? LOC_V : LOC_L); }
constexpr int d_idx(int u, int t) {
  int n = 0;
  for (int i = 0; i < u * 10 + t; ++i) n += d_loc(i / 10, i % 10) == d_loc(u, t) ? 1 : 0;
  return n;
}
constexpr int D_RED = 4 * 28 * 1024;
static_assert(D_RED + 4 * 32 * 336 <= L_FLAG && D_RED + 4 * 16 * 164 * 4 <= L_FLAG, "lds");
DEV void ffn2_role(const Params& p, int q, int mt, LAS unsigned char* L) {
  const int rot = 3 * q;
  auto ru = [&](int u) { const int v = u + rot; return v >= 12 ? v - 12 : v; };
  const int tid = threadIdx.x, lane = tid & 63, w = __builtin_amdgcn_readfirstlane(tid >> 6);
  const int nl = lane & 15, g = lane >> 4;
  unsigned* ctrl = (unsigned*)(p.ws + OFF_CTRL);
  const rsrc_t rws = mkrsrc(p.ws, (unsigned)WS_END);
  const rsrc_t ro = mkrsrc(p.out, S_ * D_ * 2);
  const int wl = w * 28 * 1024 + lane * 16;
  claim_agprs();
  u32x4 wv[28];
  {
    const rsrc_t rd = mkrsrc(p.wd, D_ * DF * 2);
    int voff[5], sw[5];
#pragma unroll
    for (int i = 0; i < 5; ++i) {
      const int un = lane + 64 * i, r = un / 20, cu = un % 20;
      voff[i] = ((384 * w + r) * D_ + 160 * q) * 2 + cu * 16;
      sw[i] = D_RED + w * 10752 + r * 336 + cu * 16;
    }
    const int sr = D_RED + w * 10752 + (g * 8 + (nl >> 2)) * 336 + (nl & 3) * 8;
    const unsigned sra = (unsigned)(uintptr_t)(L + sr);
    constexpr int DP = 3;
    u32x4 raw[DP][10];
    auto issue = [&](int u, u32x4 (&r)[10]) {
#pragma unroll
      for (int i = 0; i < 10; ++i) r[i] = __builtin_amdgcn_raw_buffer_load_b128(rd, voff[i % 5], (32 * ru(u) + 16 * (i / 5)) * D_ * 2, 0);
    };
#pragma unroll
    for (int u = 0; u < DP - 1; ++u) issue(u, raw[u]);
    sfor<12>([&](auto U_) {
      constexpr int u = decltype(U_)::v;
      MEMFENCE();
#pragma unroll
      for (int i = 0; i < 10; ++i) lds_w128(L, sw[i % 5] + (i / 5) * 16 * 336, raw[u % DP][i]);
      MEMFENCE();
      if constexpr (u + DP - 1 < 12) issue(u + DP - 1, raw[(u + DP - 1) % DP]);
      MEMFENCE();
      sfor<10>([&](auto T_) {
        constexpr int t = decltype(T_)::v, I = d_idx(u, t);
        if constexpr (d_loc(u, t) == LOC_A) tr_to_agpr<I * 4, t * 32, 1344>(sra);
        else if constexpr (d_loc(u, t) == LOC_V) { wv[I] = tr_frag<1344>(L, sr + t * 32); pin4(wv[I]); }
        else lds_w128(L, wl + I * 1024, tr_frag<1344>(L, sr + t * 32));
      });
    });
    wg_barrier();
  }
  const int etok = tid >> 4, en = tid & 15;
  if (!wait_words(rws, PG_MOD, 40, lane, ctrl)) return;
  unsigned gbp[5];
#pragma unroll
  for (int t = 0; t < 5; ++t) gbp[t] = pack2(ldmod(rws, 5 * D_ + 160 * q + 10 * en + 2 * t), ldmod(rws, 5 * D_ + 160 * q + 10 * en + 2 * t + 1));
  const int x1o = ((en >> 3) * 16 + etok) * 16 + (en & 7) * 2;
  const rsrc_t rc = mkrsrc(comm_base(p.ws), COMM_BYTES);
  const unsigned long long selm = load_selmask(p.ws, lane);
  u32x4 a[12];
  auto lda = [&](int sb, int u0) {
#pragma unroll
    for (int u = 0; u < 12; ++u)
      if (u >= u0) a[u] = __builtin_amdgcn_raw_buffer_load_b128(rc, lane * 16, frag_off(sb, FR_ACT + mt * 48 + 12 * w + ru(u)), AUX_SC1);
  };
  int rw = D_RED + ((w * 16 + nl) * 164 + 4 * g) * 4, rr = D_RED + (etok * 164 + 10 * en) * 4;
  PIN2(rw, rr);
  if (!wait_words(rws, PG_FILL, 40, lane, ctrl)) return;
  f32x4 acc[10];
  unsigned xv[10], spins = 0;
  bool pend = false;
  for (int s = 0;;) {
    const int sb = step_base(selm, s < NSEG ? s : NSEG - 1);
    MEMFENCE();
    lda(sb, 0);
    MEMFENCE();
    if (pend) {
      pend = false;
      wg_barrier();
      asm volatile("s_nop 7" ::: "memory");
#pragma unroll
      for (int t = 0; t < 10; ++t) {
        PIN1(acc[t]);
        *(LAS f32x4*)(L + rw + t * 64) = acc[t];
      }
      wg_barrier();
      unsigned o[5];
#pragma unroll
      for (int t = 0; t < 5; ++t) {
        f32x2 v = {0.f, 0.f};
#pragma unroll
        for (int ww = 0; ww < 4; ++ww) v += *(LAS f32x2*)(L + rr + ww * (16 * 164 * 4) + t * 8);
        o[t] = pack2(bf2f(xv[2 * t]) + bflo(gbp[t]) * v.x, bf2f(xv[2 * t + 1]) + bfhi(gbp[t]) * v.y);
      }
      const int oo = (((s - 1) * 32 + mt * 16 + etok) * D_ + 160 * q + 10 * en) * 2;
      st128(u32x4{o[0], o[1], o[2], o[3]}, ro, oo, 0, 0);
      __builtin_amdgcn_raw_buffer_store_b32(o[4], ro, oo + 16, 0, 0);
      if (s == NSEG) break;
    }
    unsigned m = 0;
#pragma unroll
    for (int u = 0; u < 12; ++u) m = umax4(m, a[u]);
    if (!all_valid(m)) {
      if (++spins > SPIN_CAP) { ctrl[0] = 11u; return; }
      __builtin_amdgcn_s_sleep(1);
      continue;
    }
    MEMFENCE();
#pragma unroll
    for (int t = 0; t < 10; ++t)
      xv[t] = (unsigned)__builtin_amdgcn_raw_buffer_load_b16(rc, x1o + (t & 1) * 512, frag_off(sb, FR_X1 + mt * 20 + 5 * q + (t >> 1)), AUX_SC1);
    MEMFENCE();
    u32x4 lw[3];
    sfor<12>([&](auto U_) {
      constexpr int u = decltype(U_)::v;
      sfor<10>([&](auto T_) {
        constexpr int t = decltype(T_)::v, I = d_idx(u, t), IL = d_idx(u, t < 3 ? 7 + t : 9);
        if constexpr (d_loc(u, t) == LOC_A) mfa<I * 4, u == 0>(acc[t], a[u]);
        else if constexpr (d_loc(u, t) == LOC_V) mfv2<u == 0>(acc[t], wv[I], a[u]);
        else mfv2<u == 0>(acc[t], lw[t - 7], a[u]);
        if constexpr (t < 3 && d_loc(u, 7 + t) == LOC_L) lw[t] = lds_r128(L, wl + IL * 1024);
      });
    });
    asm volatile("s_nop 7" ::: "memory");
#pragma unroll
    for (int t = 0; t < 10; ++t) PIN2(xv[t], acc[t]);
    pend = true;
    spins = 0;
    ++s;
  }
}

__global__ void __launch_bounds__(256, 1) k_mega(Params p) {
  extern __shared__ __attribute__((aligned(16))) unsigned char smem[];
  LAS unsigned char* L = (LAS unsigned char*)smem;
  if (threadIdx.x == 0) {
    unsigned x = xcc_id() & 7u, t;
    for (;;) {
      t = __hip_atomic_fetch_add((unsigned*)(p.ws + OFF_CTRL) + CTRL_TICKET + x * 32, 1u, __ATOMIC_RELAXED, __HIP_MEMORY_SCOPE_AGENT);
      if (t < 32u) break;
      x = (x + 1u) & 7u;
    }
    *(LAS int*)(L + L_FLAG + 32) = (int)((x >> 2) * 128u + t * 4u + (x & 3u));
  }
  __syncthreads();
  const int b = __builtin_amdgcn_readfirstlane(*(LAS int*)(L + L_FLAG + 32));
  if (b >= 64 && b < 104) task_fill_early(p, b - 64);
  if (b >= 64 && b < 128) task_pull(p, b & 3, (b - 64) >> 2);
  if (b >= 192) { if (!task_mod_ln(p, b - 192, L)) return; }
  if (b >= 192 || (b >= 64 && b < 128)) { if (!wait_startup(mkrsrc(p.ws, (unsigned)WS_END), (unsigned*)(p.ws + OFF_CTRL))) return; }
  if (b < 32) qk_role(p, b, L);
  else if (b < 64) v_role(p, b - 32, L);
  else if (b < 104) col_role(p, b - 64, L);
  else if (b < 128) {
    const int r = (b - 104) >> 2, x = b & 3;
    if (r < 4 && x < 2) ffn2_role(p, r, x, L);
    else px_role(p, r < 4 ? r * 2 + x - 2 : 8 + (r - 4) * 4 + x, L);
  }
  else if (b < 160) vox_role(p, b - 128, L);
  else if (b < 192) qkx_role(p, b - 160, L);
  else if (b < 232) xcol_role(p, (b - 192) >> 1, (b - 192) & 1, L);
  else ffn1_role(p, (b - 232) >> 1, (b - 232) & 1, L);
}

extern "C" void kernel_launch(void* const* d_in, const int* in_sizes, int n_in, void* d_out, int out_size, void* d_ws, size_t ws_size,
                              hipStream_t stream) {
  Params p;
  p.x = (const u16*)d_in[0]; p.mem0 = (const u16*)d_in[1]; p.wq = (const u16*)d_in[2]; p.wk = (const u16*)d_in[3];
  p.wv = (const u16*)d_in[4]; p.wo = (const u16*)d_in[5]; p.wg = (const u16*)d_in[8]; p.wu = (const u16*)d_in[9];
  p.wd = (const u16*)d_in[10]; p.c = (const u16*)d_in[11]; p.wmod = (const u16*)d_in[12];
  p.out = (u16*)d_out; p.ws = (unsigned char*)d_ws;
  constexpr int LDSB = L_END;
  (void)hipFuncSetAttribute((const void*)k_mega, hipFuncAttributeMaxDynamicSharedMemorySize, LDSB);
  k_init<<<65 + 80, 256, 0, stream>>>(p);
  k_mega<<<256, 256, LDSB, stream>>>(p);
}
```
